```python
import torch

def _softmax(x, axis=-1):
    return torch.softmax(x, dim=axis)
def _gelu(x):
    return torch.nn.functional.gelu(x, approximate="tanh")
def _silu(x):
    return torch.nn.functional.silu(x)
def _sigmoid(x):
    return torch.sigmoid(x)
def _layernorm(x, axis=-1, eps=1e-5):
    m = x.mean(dim=axis, keepdim=True)
    v = x.var(dim=axis, keepdim=True, unbiased=False)
    return (x - m) / torch.sqrt(v + eps)
def _scatter_add(tgt, idx, src, axis=0):
    return tgt.clone().index_add_(axis, idx, src)
def _segment_sum(src, seg, n):
    out = torch.zeros((n,) + src.shape[1:], dtype=src.dtype)
    return out.index_add_(0, seg, src)
def _pad(x, pw):
    flat = tuple(v for pair in reversed(pw) for v in pair)
    return torch.nn.functional.pad(x, flat)

kernel_name = 'sequential_preLN__softmax_full_mha__geglu_ffn__prompt'

def setup_inputs():

    _w = (lambda a: (lambda t: t.to(torch.bfloat16) if t.is_floating_point() else t)(torch.as_tensor(a)))
    def _A_setup():
        B, S, D = 1, 2048, 512
        return {
            "x": _w((lambda *s: torch.randn(*s, dtype=torch.bfloat16) * 0.05)(B, S, D)),
            "wqkv": _w((lambda *s: torch.randn(*s, dtype=torch.bfloat16) * 0.05)(D, 3 * D)),
            "wo": _w((lambda *s: torch.randn(*s, dtype=torch.bfloat16) * 0.05)(D, D)),
        }
    _A_in = _A_setup()

    def _B_setup():
        B, S, D, F = 1, 2048, 512, 1792
        return {
            "x": _w((lambda *s: torch.randn(*s, dtype=torch.bfloat16) * 0.05)(B, S, D)),
            "w_gate": _w((lambda *s: torch.randn(*s, dtype=torch.bfloat16) * 0.05)(D, F)),
            "w_up": _w((lambda *s: torch.randn(*s, dtype=torch.bfloat16) * 0.05)(D, F)),
            "w_down": _w((lambda *s: torch.randn(*s, dtype=torch.bfloat16) * 0.05)(F, D)),
        }
    _B_in = _B_setup()

    return {
        "A_x": _A_in["x"],
        "A_wqkv": _A_in["wqkv"],
        "A_wo": _A_in["wo"],
        "B_x": _B_in["x"],
        "B_w_gate": _B_in["w_gate"],
        "B_w_up": _B_in["w_up"],
        "B_w_down": _B_in["w_down"],
    }

def reference(A_x, A_wqkv, A_wo, B_x, B_w_gate, B_w_up, B_w_down):
    def _A_ref(x, wqkv, wo):
        B, S, D = x.shape
        H = 32; Dh = D // H
        qkv = (lambda x, *d: x.permute(*d))((x @ wqkv).reshape(B, S, 3, H, Dh), 2, 0, 3, 1, 4)
        q, k, v = qkv[0], qkv[1], qkv[2]
        scale = Dh ** -0.5
        scores = torch.einsum("bhqd,bhkd->bhqk", q, k) * scale
        p = _softmax(scores, axis=-1)
        o = torch.einsum("bhqk,bhkd->bhqd", p, v)
        return (lambda x, *d: x.permute(*d))(o, 0, 2, 1, 3).reshape(B, S, D) @ wo

    def _B_ref(x, w_gate, w_up, w_down):
        return (_gelu(x @ w_gate) * (x @ w_up)) @ w_down

    x = A_x
    h = x + _A_ref(_layernorm(x, axis=-1), A_wqkv, A_wo)
    h2 = h + _B_ref(_layernorm(h, axis=-1), B_w_gate, B_w_up, B_w_down)
    return h2
```

```cpp
#include <hip/hip_runtime.h>
#include <hip/hip_bf16.h>
#include <stdint.h>

typedef unsigned short u16;
typedef __attribute__((ext_vector_type(4))) unsigned u32x4;
typedef __attribute__((ext_vector_type(2))) unsigned u32x2;
typedef __attribute__((ext_vector_type(8))) __bf16 bf16x8;
typedef __attribute__((ext_vector_type(16))) __bf16 bf16x16;
typedef __attribute__((ext_vector_type(2))) __bf16 bf16x2;
typedef __attribute__((ext_vector_type(16))) float f32x16;
typedef __attribute__((ext_vector_type(8))) float f32x8;
typedef __attribute__((ext_vector_type(4))) float f32x4;
typedef __attribute__((ext_vector_type(2))) float f32x2;
typedef __attribute__((ext_vector_type(4))) short s16x4;
#define LAS __attribute__((address_space(3)))

constexpr int SEQ = 2048, DM = 512, NH = 32, FF = 1792;
constexpr float LN_EPS = 1e-5f;
constexpr float QSCALE = 0.25f * 1.4426950408889634f;

constexpr size_t WS_Q = 0;
constexpr size_t WS_K = WS_Q + (2u << 20);
constexpr size_t WS_V = WS_K + (2u << 20);
constexpr size_t WS_O = WS_V + (2u << 20);
constexpr size_t WS_H = WS_O + (2u << 20);
constexpr size_t WS_A = WS_H + (2u << 20);
constexpr size_t WS_WO = WS_A + (size_t)64 * 112 * 1024;
constexpr size_t WS_WGU = WS_WO + (size_t)16 * 32 * 1024;
constexpr size_t WS_WD = WS_WGU + (size_t)112 * 32 * 1024;
constexpr size_t WS_CS = WS_WD + (size_t)16 * 112 * 1024;
constexpr size_t WS_ST = WS_CS + (size_t)112 * 8 * 32 * 4;
constexpr size_t WS_END = WS_ST + (size_t)2048 * 16 * 2 * 4;

__device__ __forceinline__ f32x16 mfma32(u32x4 a, u32x4 b, f32x16 c) {
    return __builtin_amdgcn_mfma_f32_32x32x16_bf16(__builtin_bit_cast(bf16x8, a), __builtin_bit_cast(bf16x8, b), c, 0, 0, 0);
}
__device__ __forceinline__ unsigned pk_bf16(float lo, float hi) {
    f32x2 v = {lo, hi};
    return __builtin_bit_cast(unsigned, __builtin_convertvector(v, bf16x2));
}
__device__ __forceinline__ float bf_lo(unsigned u) { return __uint_as_float(u << 16); }
__device__ __forceinline__ float bf_hi(unsigned u) { return __uint_as_float(u & 0xffff0000u); }
__device__ __forceinline__ float dot2(unsigned a, unsigned b, float c) {
    return __builtin_amdgcn_fdot2_f32_bf16(__builtin_bit_cast(bf16x2, a), __builtin_bit_cast(bf16x2, b), c, false);
}
__device__ __forceinline__ float xor32(float v) {
    auto r = __builtin_amdgcn_permlane32_swap(__float_as_uint(v), __float_as_uint(v), false, false);
    return (threadIdx.x & 32) ? __uint_as_float(r[0]) : __uint_as_float(r[1]);
}
__device__ __forceinline__ float sum_halves(float v) {
    auto r = __builtin_amdgcn_permlane32_swap(__float_as_uint(v), __float_as_uint(v), false, false);
    return __uint_as_float(r[0]) + __uint_as_float(r[1]);
}
__device__ __forceinline__ float max_halves(float v) {
    auto r = __builtin_amdgcn_permlane32_swap(__float_as_uint(v), __float_as_uint(v), false, false);
    return fmaxf(__uint_as_float(r[0]), __uint_as_float(r[1]));
}
__device__ __forceinline__ void glds16(const void* base, unsigned voff, unsigned lds_dst) {
    unsigned keep;
    asm volatile("s_mov_b32 %0, m0\n\ts_mov_b32 m0, %3\n\ts_nop 0\n\tglobal_load_lds_dwordx4 %1, %2\n\ts_mov_b32 m0, %0"
                 : "=&s"(keep) : "v"(voff), "s"(base), "s"(lds_dst) : "memory");
}
#define WAITV_BAR(n) asm volatile("s_waitcnt vmcnt(" #n ") lgkmcnt(0)\n\ts_barrier" ::: "memory")
#define WAITL_BAR() asm volatile("s_waitcnt lgkmcnt(0)\n\ts_barrier" ::: "memory")
__device__ __forceinline__ u32x4 lds_ld128(unsigned addr) { return *(const LAS u32x4*)addr; }
__device__ __forceinline__ f32x4 lds_ldf4(unsigned addr) { return *(const LAS f32x4*)addr; }
__device__ __forceinline__ u32x2 lds_tr(unsigned addr) {
    return __builtin_bit_cast(u32x2, __builtin_amdgcn_ds_read_tr16_b64_v4i16((LAS s16x4*)addr));
}
__device__ __forceinline__ int rfl(int v) { return __builtin_amdgcn_readfirstlane(v); }

constexpr int K1_SLOT = 28672, K1_NSLOT = 5, K1_CS = K1_SLOT * K1_NSLOT, K1_ST = K1_CS + 4 * 96 * 4, K1_LDS = K1_ST + 4 * 64 * 4;

template <int NV>
__device__ __forceinline__ void k1_compute(unsigned lds0, int w, int lane, int mt, int nt, char* ws) {
    const int r = lane & 31, h = lane >> 5;
    f32x16 acc[3];
#pragma unroll
    for (int j = 0; j < 3; ++j) acc[j] = f32x16{};
    float sx = 0.f, sxx = 0.f;
    unsigned aoff[4];
#pragma unroll
    for (int s = 0; s < 4; ++s) aoff[s] = (32 * w + r) * 128 + (((2 * s + h) ^ ((r >> 1) & 7)) * 16);
    const unsigned boff = 16384 + (8 * h + ((lane & 15) >> 2)) * 192 + (((lane >> 4) & 1) * 16 + (lane & 3) * 4) * 2;
    int slot = 0;
    for (int c = 0; c < 8; ++c) {
        WAITL_BAR();
        const unsigned sb = lds0 + slot * K1_SLOT;
#pragma unroll
        for (int s = 0; s < 4; ++s) {
            const u32x4 xa = lds_ld128(sb + aoff[s]);
            u32x4 wf[3];
#pragma unroll
            for (int j = 0; j < 3; ++j) {
                const u32x2 lo = lds_tr(sb + boff + s * 16 * 192 + j * 64), hi = lds_tr(sb + boff + s * 16 * 192 + 4 * 192 + j * 64);
                wf[j] = u32x4{lo[0], lo[1], hi[0], hi[1]};
            }
#pragma unroll
            for (int j = 0; j < 3; ++j) acc[j] = (j >= 3 - NV) ? mfma32(xa, wf[j], acc[j]) : mfma32(wf[j], xa, acc[j]);
#pragma unroll
            for (int e = 0; e < 4; ++e) { sx = dot2(xa[e], 0x3f803f80u, sx); sxx = dot2(xa[e], xa[e], sxx); }
        }
        slot = (slot == K1_NSLOT - 1) ? 0 : slot + 1;
    }
    sx = sum_halves(sx); sxx = sum_halves(sxx);
    const float mu = sx * (1.f / DM);
    const float var = fmaxf(sxx * (1.f / DM) - mu * mu, 0.f);
    const float rstd = __builtin_amdgcn_rsqf(var + LN_EPS);
    if (NV > 0) { if (h == 0) *(LAS float*)(lds0 + K1_ST + w * 256 + r * 4) = rstd; else *(LAS float*)(lds0 + K1_ST + w * 256 + 128 + r * 4) = -mu * rstd; }
    WAITL_BAR();
    const int tile = mt * 4 + w;
#pragma unroll
    for (int j = 0; j < 3; ++j) {
        const int c0 = nt * 96 + 32 * j, head = (c0 & 511) >> 4;
        if (j >= 3 - NV) {
            float cs = 0.f;
#pragma unroll
            for (int p = 0; p < 4; ++p) cs += *(const LAS float*)(lds0 + K1_CS + (p * 96 + 32 * j + r) * 4);
            unsigned o[8];
#pragma unroll
            for (int g = 0; g < 4; ++g) {
                const f32x4 rs = lds_ldf4(lds0 + K1_ST + w * 256 + (8 * g + 4 * h) * 4), nm = lds_ldf4(lds0 + K1_ST + w * 256 + 128 + (8 * g + 4 * h) * 4);
                float v[4];
#pragma unroll
                for (int e = 0; e < 4; ++e) v[e] = fmaf(acc[j][4 * g + e], rs[e], nm[e] * cs);
                o[2 * g] = pk_bf16(v[0], v[1]); o[2 * g + 1] = pk_bf16(v[2], v[3]);
            }
            u32x4* vp = (u32x4*)(ws + WS_V) + (((size_t)(head + (r >> 4)) * 128 + tile * 2) * 2 + h) * 16 + (r & 15);
            vp[0] = u32x4{o[0], o[1], o[2], o[3]};
            vp[32] = u32x4{o[4], o[5], o[6], o[7]};
        } else {
            const bool isq = c0 < 512;
            const float sc = isq ? rstd * QSCALE : rstd, nm = -mu * sc;
            unsigned o[8];
#pragma unroll
            for (int g = 0; g < 4; ++g) {
                f32x4 cs = lds_ldf4(lds0 + K1_CS + (32 * j + 8 * g + 4 * h) * 4);
#pragma unroll
                for (int p = 1; p < 4; ++p) cs += lds_ldf4(lds0 + K1_CS + (p * 96 + 32 * j + 8 * g + 4 * h) * 4);
                float v[4];
#pragma unroll
                for (int e = 0; e < 4; ++e) v[e] = fmaf(acc[j][4 * g + e], sc, nm * cs[e]);
                o[2 * g] = pk_bf16(v[0], v[1]); o[2 * g + 1] = pk_bf16(v[2], v[3]);
            }
            const int rr = isq ? r : ((r & 19) | ((r & 4) << 1) | ((r & 8) >> 1));
            u32x4* qp = (u32x4*)(ws + (isq ? WS_Q : WS_K)) + ((size_t)head * 64 + tile) * 64 + h * 32 + rr;
            qp[0] = u32x4{o[0], o[1], o[2], o[3]};
            qp[64 * 64] = u32x4{o[4], o[5], o[6], o[7]};
        }
    }
}

__global__ __launch_bounds__(512, 2) void k1_qkv(const u16* __restrict__ x, const u16* __restrict__ wqkv, char* __restrict__ ws) {
    __shared__ __attribute__((aligned(16))) char lds[K1_LDS];
    const unsigned lds0 = (unsigned)(uintptr_t)lds;
    const int b = blockIdx.x, xc = b & 7, i = b >> 3;
    const int mt = (xc >> 1) * 4 + (i & 3), nt = (xc & 1) * 8 + (i >> 2);
    const int lane = threadIdx.x & 63, w = rfl(threadIdx.x >> 6);
    if (w < 4) {
        if (nt < 10) k1_compute<0>(lds0, w, lane, mt, nt, ws);
        else if (nt == 10) k1_compute<1>(lds0, w, lane, mt, nt, ws);
        else k1_compute<3>(lds0, w, lane, mt, nt, ws);
    } else {
        const int j = w - 4;
        unsigned xo[4], wo[3];
#pragma unroll
        for (int p = 0; p < 4; ++p) { const int row = 8 * (4 * j + p) + (lane >> 3); xo[p] = row * 1024 + (((lane & 7) ^ ((row >> 1) & 7)) * 16); }
#pragma unroll
        for (int p = 0; p < 3; ++p) { const int off = (3 * j + p) * 1024 + lane * 16; wo[p] = (off / 192) * 3072 + (off % 192); }
        const char* xb = (const char*)x + (size_t)mt * 128 * 1024;
        const char* wb = (const char*)wqkv + nt * 192;
        auto issue = [&](int c, int slot) {
            const unsigned sb = lds0 + slot * K1_SLOT;
#pragma unroll
            for (int p = 0; p < 4; ++p) glds16(xb + c * 128, xo[p], sb + (4 * j + p) * 1024);
#pragma unroll
            for (int p = 0; p < 3; ++p) glds16(wb + (size_t)c * 64 * 3072, wo[p], sb + 16384 + (3 * j + p) * 1024);
        };
#pragma unroll
        for (int c = 0; c < 5; ++c) issue(c, c);
        const int h = lane >> 5;
        const unsigned boff = 16384 + (16 * j + 8 * h + ((lane & 15) >> 2)) * 192 + (((lane >> 4) & 1) * 16 + (lane & 3) * 4) * 2;
        float cs[3] = {0.f, 0.f, 0.f};
        auto colsum = [&](int slot) {
            const unsigned sb = lds0 + slot * K1_SLOT;
#pragma unroll
            for (int jj = 0; jj < 3; ++jj) {
                const u32x2 lo = lds_tr(sb + boff + jj * 64), hi = lds_tr(sb + boff + 4 * 192 + jj * 64);
                cs[jj] = dot2(lo[0], 0x3f803f80u, cs[jj]); cs[jj] = dot2(lo[1], 0x3f803f80u, cs[jj]);
                cs[jj] = dot2(hi[0], 0x3f803f80u, cs[jj]); cs[jj] = dot2(hi[1], 0x3f803f80u, cs[jj]);
            }
        };
        WAITV_BAR(28); colsum(0);
        WAITV_BAR(21); issue(5, 0); colsum(1);
        WAITV_BAR(21); issue(6, 1); colsum(2);
        WAITV_BAR(21); issue(7, 2); colsum(3);
        WAITV_BAR(21); colsum(4);
        WAITV_BAR(14); colsum(0);
        WAITV_BAR(7);  colsum(1);
        WAITV_BAR(0);  colsum(2);
#pragma unroll
        for (int jj = 0; jj < 3; ++jj) { const float t = sum_halves(cs[jj]); if (h == 0) *(LAS float*)(lds0 + K1_CS + (j * 96 + 32 * jj + lane) * 4) = t; }
        WAITL_BAR();
    }
}

constexpr int A_K = 0, A_V = 65536, A_ONE = 131072, A_X = A_ONE + 2048, A_FLAG = A_X + 8 * 9 * 256, A_LDS = A_FLAG + 16;

__device__ __forceinline__ f32x16 smf(u32x4 a, f32x8 p, f32x16 c) {
    return __builtin_amdgcn_smfmac_f32_32x32x32_bf16(__builtin_bit_cast(bf16x8, a), __builtin_bit_cast(bf16x16, p), c, (int)0xDDDDDDDDu, 0, 0);
}
__device__ __forceinline__ void exp_pv(f32x16& s, u32x4 v0, u32x4 v1, f32x16& o) {
    f32x8 p0, p1;
#pragma unroll
    for (int i = 0; i < 8; ++i) p0[i] = __builtin_amdgcn_exp2f(s[i]);
#pragma unroll
    for (int i = 0; i < 8; ++i) p1[i] = __builtin_amdgcn_exp2f(s[8 + i]);
    o = smf(v0, p0, o);
    o = smf(v1, p1, o);
}
__device__ __forceinline__ float max16(const f32x16& s) {
    float m = fmaxf(s[0], s[1]);
#pragma unroll
    for (int i = 2; i < 16; ++i) m = fmaxf(m, s[i]);
    return m;
}
__device__ __forceinline__ f32x16 splat16(float v) { f32x16 r;
#pragma unroll
    for (int i = 0; i < 16; ++i) r[i] = v;
    return r; }

__global__ __launch_bounds__(512, 2) void k2_attn(char* __restrict__ ws, const u16* __restrict__ wo, const u16* __restrict__ wg,
                                                  const u16* __restrict__ wu, const u16* __restrict__ wd) {
    __shared__ __attribute__((aligned(16))) char lds[A_LDS];
    const unsigned lds0 = (unsigned)(uintptr_t)lds;
    const int b = blockIdx.x, head = (b & 7) * 4 + ((b >> 3) & 3), qb = b >> 5;
    const int lane = threadIdx.x & 63, w = rfl(threadIdx.x >> 6), r = lane & 31, h = lane >> 5;
    const int kvh = w >> 2, qi = w & 3;
    {
        const char* src = ws + ((w & 1) ? WS_V : WS_K) + (size_t)head * 65536 + ((w >> 1) & 1) * 32768 + (w >> 2) * 1024;
        const unsigned dst = lds0 + ((w & 1) ? A_V : A_K) + ((w >> 1) & 1) * 32768 + (w >> 2) * 1024;
#pragma unroll
        for (int i = 0; i < 16; ++i) glds16(src + i * 2048, lane * 16, dst + i * 2048);
    }
    const int rt0 = qb * 8 + qi * 2;
    const u32x4* qf = (const u32x4*)(ws + WS_Q) + ((size_t)head * 64 + rt0) * 64 + lane;
    const u32x4 qA = qf[0], qB = qf[64];
    const int gw = w * 256 + b;
    const u16* wsrc; int pitch; u32x4* wdst; bool wact = gw < 1472, wcs = false; int csidx = 0;
    if (gw < 128) { const int ct = gw >> 3, kg = gw & 7; wsrc = wo + (size_t)(kg * 64) * 512 + ct * 32 + r; pitch = 512; wdst = (u32x4*)(ws + WS_WO) + (size_t)(ct * 32 + kg * 4) * 64; }
    else if (gw < 1024) { const int idx = gw - 128, ct = idx >> 3, kg = idx & 7; wsrc = (r < 16 ? wg : wu) + (size_t)(kg * 64) * FF + ct * 16 + (r & 15); pitch = FF;
        wdst = (u32x4*)(ws + WS_WGU) + (size_t)(ct * 32 + kg * 4) * 64; wcs = true; csidx = idx * 32; }
    else { const int idx = (gw < 1472 ? gw : 1024) - 1024, ct = idx / 28, kg = idx % 28; wsrc = wd + (size_t)(kg * 64) * 512 + ct * 32 + r; pitch = 512;
        wdst = (u32x4*)(ws + WS_WD) + (size_t)(ct * 112 + kg * 4) * 64; }
    u16 wv[32];
    if (wact) {
#pragma unroll
        for (int e = 0; e < 32; ++e) wv[e] = wsrc[(size_t)((e >> 3) * 16 + ((e >> 2) & 1) * 8 + 4 * h + (e & 3)) * pitch];
    }
    *(LAS unsigned*)(lds0 + A_ONE + threadIdx.x * 4) = 0x3f803f80u;
    if (threadIdx.x == 0) *(LAS unsigned*)(lds0 + A_FLAG) = 0u;

    const unsigned kbase = lds0 + A_K + kvh * 32768 + lane * 16;
    const unsigned vbase = (r < 16) ? (lds0 + A_V + kvh * 32768 + h * 256 + r * 16) : (lds0 + A_ONE + h * 256 + (r & 15) * 16);
    const unsigned vstep = (r < 16) ? 2048u : 0u;
    f32x16 cA, cB, oA, oB;
    for (int attempt = 0; attempt < 2; ++attempt) {
        if (attempt == 0) {
            asm volatile("s_waitcnt vmcnt(0) lgkmcnt(0)\n\ts_barrier" ::: "memory");
            const u32x4 k0 = lds_ld128(lds0 + A_K + lane * 16);
            const f32x16 s0 = mfma32(k0, qA, f32x16{}), s1 = mfma32(k0, qB, f32x16{});
            cA = splat16(-max_halves(max16(s0))); cB = splat16(-max_halves(max16(s1)));
            if (wact) {
                float cs = 0.f;
#pragma unroll
                for (int f = 0; f < 4; ++f) {
                    u32x4 t;
#pragma unroll
                    for (int e = 0; e < 4; ++e) { t[e] = (unsigned)wv[f * 8 + 2 * e] | ((unsigned)wv[f * 8 + 2 * e + 1] << 16); cs = dot2(t[e], 0x3f803f80u, cs); }
                    wdst[f * 64 + lane] = t;
                }
                cs = sum_halves(cs);
                if (wcs && h == 0) ((float*)(ws + WS_CS))[csidx + r] = cs;
            }
        } else {
            float mA = -3.0e38f, mB = -3.0e38f;
            for (int t = 0; t < 64; ++t) {
                const u32x4 k0 = lds_ld128(lds0 + A_K + t * 1024 + lane * 16);
                const f32x16 s0 = mfma32(k0, qA, f32x16{}), s1 = mfma32(k0, qB, f32x16{});
                mA = fmaxf(mA, max16(s0)); mB = fmaxf(mB, max16(s1));
            }
            cA = splat16(-max_halves(mA)); cB = splat16(-max_halves(mB));
        }
        oA = f32x16{}; oB = f32x16{};
        unsigned kp = kbase, vp = vbase;
        u32x4 kc = lds_ld128(kp);
        f32x16 sA = mfma32(kc, qA, cA), sB;
        for (int t = 0; t < 32; t += 2) {
            u32x4 v0 = lds_ld128(vp), v1 = lds_ld128(vp + 512), kn = lds_ld128(kp + 1024);
            sB = mfma32(kc, qB, cB);
            exp_pv(sA, v0, v1, oA);
            sA = mfma32(kn, qA, cA);
            exp_pv(sB, v0, v1, oB);
            v0 = lds_ld128(vp + 1024); v1 = lds_ld128(vp + 1536); kc = lds_ld128(kp + 2048);
            sB = mfma32(kn, qB, cB);
            exp_pv(sA, v0, v1, oA);
            sA = mfma32(kc, qA, cA);
            exp_pv(sB, v0, v1, oB);
            kp += 2048; vp += vstep;
        }
        if (attempt == 0) {
            bool ok = true;
#pragma unroll
            for (int i = 0; i < 9; ++i) ok = ok && (fabsf(oA[i]) < 1.0e30f) && (fabsf(oB[i]) < 1.0e30f);
            if (!ok) *(LAS unsigned*)(lds0 + A_FLAG) = 1u;
        }
        {
            const unsigned xw = lds0 + A_X + w * 2304 + lane * 4;
#pragma unroll
            for (int i = 0; i < 9; ++i) *(LAS float*)(xw + i * 256) = kvh ? oA[i] : oB[i];
        }
        WAITL_BAR();
        if (attempt == 1 || rfl(*(const LAS int*)(lds0 + A_FLAG)) == 0) break;
        WAITL_BAR();
    }
    {
        const unsigned xr = lds0 + A_X + (w ^ 4) * 2304 + lane * 4;
        float o[9];
#pragma unroll
        for (int i = 0; i < 9; ++i) o[i] = (kvh ? oB[i] : oA[i]) + *(const LAS float*)(xr + i * 256);
        const float rl = __builtin_amdgcn_rcpf(o[8]);
        u32x4 t;
#pragma unroll
        for (int e = 0; e < 4; ++e) t[e] = pk_bf16(o[2 * e] * rl, o[2 * e + 1] * rl);
        ((u32x4*)(ws + WS_O))[((size_t)(rt0 + kvh) * 32 + head) * 64 + lane] = t;
    }
}

template <int KS, bool FINAL>
__global__ __launch_bounds__(256, 1) void k35_gemm(char* __restrict__ ws, const u16* __restrict__ x, u16* __restrict__ out) {
    __shared__ __attribute__((aligned(16))) char lds[4 * 3 * 4096];
    const unsigned lds0 = (unsigned)(uintptr_t)lds;
    constexpr int NK = KS / 4;
    const int b = blockIdx.x, mt = (b & 7) * 4 + ((b >> 3) & 3), nt = b >> 5;
    const int lane = threadIdx.x & 63, w = rfl(threadIdx.x >> 6), r = lane & 31, h = lane >> 5;
    const u32x4* ap = (const u32x4*)(ws + (FINAL ? WS_A : WS_O));
    const u32x4* bp = (const u32x4*)(ws + (FINAL ? WS_WD : WS_WO));
    const int ra = w >> 1, cb = w & 1;
    const u32x4* a0 = ap + ((size_t)(mt * 2 + ra) * KS + w * NK) * 64 + lane;
    const u32x4* a1 = ap + ((size_t)(mt * 2 + (ra ^ 1)) * KS + w * NK) * 64 + lane;
    const u32x4* b0 = bp + ((size_t)(nt * 2 + cb) * KS + w * NK) * 64 + lane;
    const u32x4* b1 = bp + ((size_t)(nt * 2 + (cb ^ 1)) * KS + w * NK) * 64 + lane;
    constexpr int WIN = NK < 14 ? NK : 14;
    u32x4 fa0[WIN], fa1[WIN], fb0[WIN], fb1[WIN];
#pragma unroll
    for (int i = 0; i < WIN; ++i) { fa0[i] = a0[i * 64]; fb0[i] = b0[i * 64]; fa1[i] = a1[i * 64]; fb1[i] = b1[i * 64]; }
    const int rt = mt * 2 + ra, ct = nt * 2 + cb;
    u32x4 res[2]; u32x2 xr[4];
    if (FINAL) {
        const u32x4* hp = (const u32x4*)(ws + WS_H) + ((size_t)rt * 32 + ct * 2) * 64 + lane;
        res[0] = hp[0]; res[1] = hp[64];
    } else {
#pragma unroll
        for (int g = 0; g < 4; ++g) xr[g] = *(const u32x2*)(x + (size_t)(rt * 32 + r) * DM + ct * 32 + 8 * g + 4 * h);
    }
    __builtin_amdgcn_sched_barrier(0);
    f32x16 c00 = {}, c01 = {}, c10 = {}, c11 = {};
#pragma unroll
    for (int i = 0; i < NK; ++i) {
        const int s = i % WIN;
        c00 = mfma32(fb0[s], fa0[s], c00); c01 = mfma32(fb1[s], fa0[s], c01);
        c10 = mfma32(fb0[s], fa1[s], c10); c11 = mfma32(fb1[s], fa1[s], c11);
        if (i + WIN < NK) { fa0[s] = a0[(i + WIN) * 64]; fb0[s] = b0[(i + WIN) * 64]; fa1[s] = a1[(i + WIN) * 64]; fb1[s] = b1[(i + WIN) * 64]; }
        __builtin_amdgcn_sched_barrier(0);
    }
    auto put = [&](const f32x16& c, int code) {
        const unsigned p = lds0 + ((w ^ code) * 3 + code - 1) * 4096 + lane * 16;
#pragma unroll
        for (int g = 0; g < 4; ++g) *(LAS f32x4*)(p + g * 1024) = f32x4{c[4 * g], c[4 * g + 1], c[4 * g + 2], c[4 * g + 3]};
    };
    put(c01, 1); put(c10, 2); put(c11, 3);
    WAITL_BAR();
#pragma unroll
    for (int sl = 0; sl < 3; ++sl)
#pragma unroll
        for (int g = 0; g < 4; ++g) { const f32x4 t = lds_ldf4(lds0 + (w * 3 + sl) * 4096 + lane * 16 + g * 1024);
#pragma unroll
            for (int e = 0; e < 4; ++e) c00[4 * g + e] += t[e]; }
    unsigned o[8];
#pragma unroll
    for (int g = 0; g < 4; ++g) {
        const unsigned r0 = FINAL ? res[g >> 1][(g & 1) * 2] : xr[g][0], r1 = FINAL ? res[g >> 1][(g & 1) * 2 + 1] : xr[g][1];
        o[2 * g] = pk_bf16(c00[4 * g] + bf_lo(r0), c00[4 * g + 1] + bf_hi(r0));
        o[2 * g + 1] = pk_bf16(c00[4 * g + 2] + bf_lo(r1), c00[4 * g + 3] + bf_hi(r1));
    }
    if (FINAL) {
#pragma unroll
        for (int g = 0; g < 4; ++g) *(u32x2*)(out + (size_t)(rt * 32 + r) * DM + ct * 32 + 8 * g + 4 * h) = u32x2{o[2 * g], o[2 * g + 1]};
    } else {
        u32x4* hp = (u32x4*)(ws + WS_H) + ((size_t)rt * 32 + ct * 2) * 64 + lane;
        hp[0] = u32x4{o[0], o[1], o[2], o[3]}; hp[64] = u32x4{o[4], o[5], o[6], o[7]};
        float sx = 0.f, sxx = 0.f;
#pragma unroll
        for (int e = 0; e < 8; ++e) { sx = dot2(o[e], 0x3f803f80u, sx); sxx = dot2(o[e], o[e], sxx); }
        sx = sum_halves(sx); sxx = sum_halves(sxx);
        if (h == 0) *(f32x2*)((float*)(ws + WS_ST) + ((size_t)(rt * 32 + r) * 16 + ct) * 2) = f32x2{sx, sxx};
    }
}

constexpr int K4_SLOT = 22 * 1024, K4_NSLOT = 7, K4_CS = K4_SLOT * K4_NSLOT, K4_LDS = K4_CS + 224 * 4;

template <int NC, int C0>
__device__ __forceinline__ void k4_body(unsigned lds0, int w, int lane, int mt, int fs, char* ws) {
    const int r = lane & 31, h = lane >> 5, rtl = w & 3;
    const char* src[3]; unsigned dsto[3];
#pragma unroll
    for (int p = 0; p < 3; ++p) {
        int q = w + 8 * p; if (q >= 22) q = w;
        const int ks = q / 11, u = q % 11;
        src[p] = (u < 4) ? ws + WS_H + ((size_t)(mt * 4 + u) * 32 + ks) * 1024 : ws + WS_WGU + ((size_t)(fs * 7 + u - 4) * 32 + ks) * 1024;
        dsto[p] = q * 1024;
    }
    auto issue = [&](int c, int slot) {
#pragma unroll
        for (int p = 0; p < 3; ++p) glds16(src[p] + c * 2048, lane * 16, lds0 + slot * K4_SLOT + dsto[p]);
    };
    const int tok = (mt * 4 + rtl) * 32 + r;
    f32x4 st[4];
#pragma unroll
    for (int i = 0; i < 4; ++i) st[i] = *(const f32x4*)((const float*)(ws + WS_ST) + (size_t)tok * 32 + h * 16 + i * 4);
    float csp[8];
    if (threadIdx.x < 224) {
#pragma unroll
        for (int p = 0; p < 8; ++p) csp[p] = ((const float*)(ws + WS_CS))[((size_t)(fs * 7 + (threadIdx.x >> 5)) * 8 + p) * 32 + (threadIdx.x & 31)];
    }
    float sx = 0.f, sxx = 0.f;
#pragma unroll
    for (int i = 0; i < 4; ++i) { sx += st[i][0] + st[i][2]; sxx += st[i][1] + st[i][3]; }
    if (threadIdx.x < 224) *(LAS float*)(lds0 + K4_CS + threadIdx.x * 4) = ((csp[0] + csp[1]) + (csp[2] + csp[3])) + ((csp[4] + csp[5]) + (csp[6] + csp[7]));
    sx = sum_halves(sx); sxx = sum_halves(sxx);
    const float mu = sx * (1.f / DM);
    const float rstd = __builtin_amdgcn_rsqf(fmaxf(sxx * (1.f / DM) - mu * mu, 0.f) + LN_EPS);
    const float nmr = -mu * rstd;
#pragma unroll
    for (int c = 0; c < K4_NSLOT; ++c) issue(c, c);
    f32x16 acc[NC];
#pragma unroll
    for (int j = 0; j < NC; ++j) acc[j] = f32x16{};
    int slot = 0;
#pragma unroll 1
    for (int c = 0; c < 16; ++c) {
        if (c == 0) WAITV_BAR(18);
        else if (c <= 10) WAITV_BAR(15);
        else if (c == 11) WAITV_BAR(12);
        else if (c == 12) WAITV_BAR(9);
        else if (c == 13) WAITV_BAR(6);
        else if (c == 14) WAITV_BAR(3);
        else WAITV_BAR(0);
        if (c >= 1 && c + 6 < 16) issue(c + 6, slot == 0 ? K4_NSLOT - 1 : slot - 1);
        const unsigned sb = lds0 + slot * K4_SLOT + lane * 16;
#pragma unroll
        for (int ks = 0; ks < 2; ++ks) {
            const u32x4 a = lds_ld128(sb + (ks * 11 + rtl) * 1024);
#pragma unroll
            for (int j = 0; j < NC; ++j) acc[j] = mfma32(lds_ld128(sb + (ks * 11 + 4 + C0 + j) * 1024), a, acc[j]);
        }
        slot = (slot == K4_NSLOT - 1) ? 0 : slot + 1;
    }
    constexpr float C1 = -2.0f * 0.7978845608028654f * 1.4426950408889634f, C2 = C1 * 0.044715f;
#pragma unroll
    for (int j = 0; j < NC; ++j) {
        float a[8];
#pragma unroll
        for (int g = 0; g < 2; ++g) {
            const f32x4 cg = lds_ldf4(lds0 + K4_CS + ((C0 + j) * 32 + 8 * g + 4 * h) * 4), cu = lds_ldf4(lds0 + K4_CS + ((C0 + j) * 32 + 16 + 8 * g + 4 * h) * 4);
#pragma unroll
            for (int e = 0; e < 4; ++e) {
                const float gt = fmaf(acc[j][4 * g + e], rstd, nmr * cg[e]), up = fmaf(acc[j][8 + 4 * g + e], rstd, nmr * cu[e]);
                const float ex = __builtin_amdgcn_exp2f(gt * fmaf(gt * gt, C2, C1));
                a[4 * g + e] = gt * up * __builtin_amdgcn_rcpf(1.0f + ex);
            }
        }
        ((u32x4*)(ws + WS_A))[((size_t)(mt * 4 + rtl) * 112 + fs * 7 + C0 + j) * 64 + lane] =
            u32x4{pk_bf16(a[0], a[1]), pk_bf16(a[2], a[3]), pk_bf16(a[4], a[5]), pk_bf16(a[6], a[7])};
    }
}

__global__ __launch_bounds__(512, 2) void k4_ffn1(char* __restrict__ ws) {
    __shared__ __attribute__((aligned(16))) char lds[K4_LDS];
    const unsigned lds0 = (unsigned)(uintptr_t)lds;
    const int b = blockIdx.x, xc = b & 7, i = b >> 3;
    const int mt = (xc >> 2) * 8 + (i & 7), fs = (xc & 3) * 4 + (i >> 3);
    const int lane = threadIdx.x & 63, w = rfl(threadIdx.x >> 6);
    if (w < 4) k4_body<4, 0>(lds0, w, lane, mt, fs, ws);
    else k4_body<3, 4>(lds0, w, lane, mt, fs, ws);
}

extern "C" void kernel_launch(void* const* d_in, const int* in_sizes, int n_in, void* d_out, int out_size, void* d_ws, size_t ws_size,
                              hipStream_t stream) {
    const u16* x = (const u16*)d_in[0];
    const u16* wqkv = (const u16*)d_in[1];
    const u16* wo = (const u16*)d_in[2];
    const u16* wg = (const u16*)d_in[4];
    const u16* wu = (const u16*)d_in[5];
    const u16* wd = (const u16*)d_in[6];
    char* ws = (char*)d_ws;
    k1_qkv<<<256, 512, 0, stream>>>(x, wqkv, ws);
    k2_attn<<<256, 512, 0, stream>>>(ws, wo, wg, wu, wd);
    k35_gemm<32, false><<<256, 256, 0, stream>>>(ws, x, (u16*)d_out);
    k4_ffn1<<<256, 512, 0, stream>>>(ws);
    k35_gemm<112, true><<<256, 256, 0, stream>>>(ws, x, (u16*)d_out);
}
```

```cpp
#include <hip/hip_runtime.h>
#include <hip/hip_bf16.h>
#include <stdint.h>

typedef unsigned short u16;
typedef __attribute__((ext_vector_type(4))) unsigned u32x4;
typedef __attribute__((ext_vector_type(2))) unsigned u32x2;
typedef __attribute__((ext_vector_type(8))) __bf16 bf16x8;
typedef __attribute__((ext_vector_type(16))) __bf16 bf16x16;
typedef __attribute__((ext_vector_type(2))) __bf16 bf16x2;
typedef __attribute__((ext_vector_type(16))) float f32x16;
typedef __attribute__((ext_vector_type(8))) float f32x8;
typedef __attribute__((ext_vector_type(4))) float f32x4;
typedef __attribute__((ext_vector_type(2))) float f32x2;
typedef __attribute__((ext_vector_type(4))) short s16x4;
#define LAS __attribute__((address_space(3)))

constexpr int SEQ = 2048, DM = 512, NH = 32, FF = 1792;
constexpr float LN_EPS = 1e-5f;
constexpr float QSCALE = 0.25f * 1.4426950408889634f;

constexpr size_t WS_Q = 0;
constexpr size_t WS_K = WS_Q + (2u << 20);
constexpr size_t WS_V = WS_K + (2u << 20);
constexpr size_t WS_O = WS_V + (2u << 20);
constexpr size_t WS_H = WS_O + (2u << 20);
constexpr size_t WS_A = WS_H + (2u << 20);
constexpr size_t WS_WO = WS_A + (size_t)64 * 112 * 1024;
constexpr size_t WS_WGU = WS_WO + (size_t)16 * 32 * 1024;
constexpr size_t WS_WD = WS_WGU + (size_t)112 * 32 * 1024;
constexpr size_t WS_NQ = WS_WD + (size_t)16 * 112 * 1024;
constexpr size_t WS_NK = WS_NQ + 32 * 64 * 4;
constexpr size_t WS_CNT = WS_NK + 32 * 64 * 4;
constexpr size_t WS_ST = WS_CNT + 64 * 128;
constexpr size_t WS_END = WS_ST + (size_t)64 * 8192;

__device__ __forceinline__ f32x16 mfma32(u32x4 a, u32x4 b, f32x16 c) {
    return __builtin_amdgcn_mfma_f32_32x32x16_bf16(__builtin_bit_cast(bf16x8, a), __builtin_bit_cast(bf16x8, b), c, 0, 0, 0);
}
__device__ __forceinline__ unsigned pk_bf16(float lo, float hi) {
    f32x2 v = {lo, hi};
    return __builtin_bit_cast(unsigned, __builtin_convertvector(v, bf16x2));
}
__device__ __forceinline__ float bf_lo(unsigned u) { return __uint_as_float(u << 16); }
__device__ __forceinline__ float bf_hi(unsigned u) { return __uint_as_float(u & 0xffff0000u); }
__device__ __forceinline__ float dot2(unsigned a, unsigned b, float c) {
    return __builtin_amdgcn_fdot2_f32_bf16(__builtin_bit_cast(bf16x2, a), __builtin_bit_cast(bf16x2, b), c, false);
}
__device__ __forceinline__ float xor32(float v) {
    auto r = __builtin_amdgcn_permlane32_swap(__float_as_uint(v), __float_as_uint(v), false, false);
    return (threadIdx.x & 32) ? __uint_as_float(r[0]) : __uint_as_float(r[1]);
}
__device__ __forceinline__ float sum_halves(float v) {
    auto r = __builtin_amdgcn_permlane32_swap(__float_as_uint(v), __float_as_uint(v), false, false);
    return __uint_as_float(r[0]) + __uint_as_float(r[1]);
}
__device__ __forceinline__ float max_halves(float v) {
    auto r = __builtin_amdgcn_permlane32_swap(__float_as_uint(v), __float_as_uint(v), false, false);
    return fmaxf(__uint_as_float(r[0]), __uint_as_float(r[1]));
}
__device__ __forceinline__ void glds16(const void* base, unsigned voff, unsigned lds_dst) {
    unsigned keep;
    asm volatile("s_mov_b32 %0, m0\n\ts_mov_b32 m0, %3\n\ts_nop 0\n\tglobal_load_lds_dwordx4 %1, %2\n\ts_mov_b32 m0, %0"
                 : "=&s"(keep) : "v"(voff), "s"(base), "s"(lds_dst) : "memory");
}
#define WAITV_BAR(n) asm volatile("s_waitcnt vmcnt(" #n ") lgkmcnt(0)\n\ts_barrier" ::: "memory")
#define WAITL_BAR() asm volatile("s_waitcnt lgkmcnt(0)\n\ts_barrier" ::: "memory")
#pragma clang diagnostic ignored "-Wint-to-pointer-cast"
__device__ __forceinline__ u32x4 lds_ld128(unsigned addr) { return *(const LAS u32x4*)addr; }
__device__ __forceinline__ f32x4 lds_ldf4(unsigned addr) { return *(const LAS f32x4*)addr; }
__device__ __forceinline__ u32x2 lds_tr(unsigned addr) {
    return __builtin_bit_cast(u32x2, __builtin_amdgcn_ds_read_tr16_b64_v4i16((LAS s16x4*)addr));
}
#define STAMP_AT(ws, kid, idx) do {} while (0)
__device__ __forceinline__ int rfl(int v) { return __builtin_amdgcn_readfirstlane(v); }
#define SPIN_LOG 17
constexpr int SPIN_MAX = 1 << SPIN_LOG;
__device__ __forceinline__ void lds_wait_ge(unsigned addr, int n) {
    int spin = 0, v;
#pragma nounroll
    do {
        v = rfl(*(volatile LAS int*)addr);
        ++spin;
    } while ((v < n) & (spin < (SPIN_MAX << 6)));
    asm volatile("" ::: "memory");
}
__device__ __forceinline__ void lds_signal(unsigned addr) {
    asm volatile("s_waitcnt lgkmcnt(0)" ::: "memory");
    if ((threadIdx.x & 63) == 0) __hip_atomic_fetch_add((LAS int*)addr, 1, __ATOMIC_RELAXED, __HIP_MEMORY_SCOPE_WORKGROUP);
    asm volatile("" ::: "memory");
}

constexpr int K1_SLOT = 28672, K1_NSLOT = 5, K1_CP = K1_SLOT * K1_NSLOT, K1_CS = K1_CP + 4 * 512, K1_ST = K1_CS + 4 * 512, K1_LDS = K1_ST + 4 * 256;

template <int NV>
__device__ __forceinline__ void k1_compute(unsigned lds0, int w, int lane, int mt, int nt, char* ws) {
    const int r = lane & 31, h = lane >> 5;
    f32x16 acc[3]; f32x4 cs[3];
#pragma unroll
    for (int j = 0; j < 3; ++j) { acc[j] = f32x16{}; cs[j] = f32x4{}; }
    const unsigned one = ((lane & 15) == ((lane >> 4) & 1)) ? 0x3f803f80u : 0u;
    const u32x4 sel = {one, one, one, one};
    const unsigned boff = 16384 + (8 * h + ((lane & 15) >> 2)) * 192 + (((lane >> 4) & 1) * 16 + (lane & 3) * 4) * 2;
    u32x4 xa[4] = {}, wf[4][3] = {};
    auto rd = [&](unsigned sb, int s) {
        const int sr = (s + w) & 3;
        xa[s] = lds_ld128(sb + (32 * w + r) * 128 + (((2 * sr + h) ^ ((r >> 1) & 7)) * 16));
#pragma unroll
        for (int j = 0; j < 3; ++j) {
            const u32x2 lo = lds_tr(sb + boff + sr * 16 * 192 + j * 64), hi = lds_tr(sb + boff + sr * 16 * 192 + 4 * 192 + j * 64);
            wf[s][j] = u32x4{lo[0], lo[1], hi[0], hi[1]};
        }
    };
    auto mm = [&](int s) {
#pragma unroll
        for (int j = 0; j < 3; ++j) acc[j] = (j >= 3 - NV) ? mfma32(xa[s], wf[s][j], acc[j]) : mfma32(wf[s][j], xa[s], acc[j]);
    };
    int slot = 0;
#pragma nounroll
    for (int c = 0; c < 8; ++c) {
        WAITL_BAR();
        if (c == 0 || c == 4) STAMP_AT(ws, 0, 1 + c);
        const unsigned sb = lds0 + slot * K1_SLOT;
        rd(sb, 0); rd(sb, 1);
        mm(2); mm(3);
#pragma unroll
        for (int g = 0; g < 2; ++g) { __builtin_amdgcn_sched_group_barrier(0x008, 1, 0); __builtin_amdgcn_sched_group_barrier(0x100, 3, 0); }
#pragma unroll
        for (int g = 0; g < 4; ++g) { __builtin_amdgcn_sched_group_barrier(0x008, 1, 0); __builtin_amdgcn_sched_group_barrier(0x100, 2, 0); }
        __builtin_amdgcn_sched_barrier(0);
        rd(sb, 2); rd(sb, 3);
        mm(0); mm(1);
#pragma unroll
        for (int j = 0; j < 3; ++j) cs[j] = __builtin_amdgcn_mfma_f32_16x16x32_bf16(__builtin_bit_cast(bf16x8, sel), __builtin_bit_cast(bf16x8, wf[0][j]), cs[j], 0, 0, 0);
#pragma unroll
        for (int g = 0; g < 7; ++g) { __builtin_amdgcn_sched_group_barrier(0x008, 1, 1); __builtin_amdgcn_sched_group_barrier(0x100, 2, 1); }
        __builtin_amdgcn_sched_barrier(0);
        slot = (slot == K1_NSLOT - 1) ? 0 : slot + 1;
    }
    mm(2); mm(3);
    STAMP_AT(ws, 0, 10);
    if (lane < 16) {
#pragma unroll
        for (int j = 0; j < 3; ++j) { *(LAS float*)(lds0 + K1_CP + w * 512 + (32 * j + lane) * 4) = cs[j][0]; *(LAS float*)(lds0 + K1_CP + w * 512 + (32 * j + 16 + lane) * 4) = cs[j][1]; }
    }
    WAITL_BAR();
    const unsigned cst = lds0 + K1_CS + w * 512;
    {
        f32x2 t = *(const LAS f32x2*)(lds0 + K1_CP + lane * 8);
#pragma unroll
        for (int p = 1; p < 4; ++p) t += *(const LAS f32x2*)(lds0 + K1_CP + p * 512 + lane * 8);
        *(LAS f32x2*)(cst + lane * 8) = t;
        asm volatile("s_waitcnt lgkmcnt(0)" ::: "memory");
    }
    const float rstd = *(const LAS float*)(lds0 + K1_ST + w * 256 + r * 4), nmr = *(const LAS float*)(lds0 + K1_ST + w * 256 + 128 + r * 4);
    const int tile = mt * 4 + w;
#pragma unroll
    for (int j = 0; j < 3; ++j) {
        const int c0 = nt * 96 + 32 * j, head = (c0 & 511) >> 4;
        if (j >= 3 - NV) {
            const float csv = *(const LAS float*)(cst + (32 * j + r) * 4);
            unsigned o[8];
#pragma unroll
            for (int g = 0; g < 4; ++g) {
                const f32x4 rs = lds_ldf4(lds0 + K1_ST + w * 256 + (8 * g + 4 * h) * 4), nm = lds_ldf4(lds0 + K1_ST + w * 256 + 128 + (8 * g + 4 * h) * 4);
                float v[4];
#pragma unroll
                for (int e = 0; e < 4; ++e) v[e] = fmaf(acc[j][4 * g + e], rs[e], nm[e] * csv);
                o[2 * g] = pk_bf16(v[0], v[1]); o[2 * g + 1] = pk_bf16(v[2], v[3]);
            }
            u32x4* vp = (u32x4*)(ws + WS_V) + (((size_t)(head + (r >> 4)) * 128 + tile * 2) * 2 + h) * 16 + (r & 15);
            vp[0] = u32x4{o[0], o[1], o[2], o[3]};
            vp[32] = u32x4{o[4], o[5], o[6], o[7]};
        } else {
            const bool isq = c0 < 512;
            const float qs = isq ? QSCALE : 1.0f, sc = rstd * qs, nm = nmr * qs;
            unsigned o[8];
#pragma unroll
            for (int g = 0; g < 4; ++g) {
                const f32x4 c4 = lds_ldf4(cst + (32 * j + 8 * g + 4 * h) * 4);
                float v[4];
#pragma unroll
                for (int e = 0; e < 4; ++e) v[e] = fmaf(acc[j][4 * g + e], sc, nm * c4[e]);
                o[2 * g] = pk_bf16(v[0], v[1]); o[2 * g + 1] = pk_bf16(v[2], v[3]);
            }
            const int rr = isq ? r : ((r & 19) | ((r & 4) << 1) | ((r & 8) >> 1));
            u32x4* qp = (u32x4*)(ws + (isq ? WS_Q : WS_K)) + ((size_t)head * 64 + tile) * 64 + h * 32 + rr;
            qp[0] = u32x4{o[0], o[1], o[2], o[3]};
            qp[64 * 64] = u32x4{o[4], o[5], o[6], o[7]};
            float n0 = 0.f, n1 = 0.f;
#pragma unroll
            for (int e = 0; e < 4; ++e) { n0 = dot2(o[e], o[e], n0); n1 = dot2(o[4 + e], o[4 + e], n1); }
            n0 = sum_halves(n0); n1 = sum_halves(n1);
            unsigned nn = __float_as_uint(h ? n1 : n0);
            nn = max(nn, (unsigned)__builtin_amdgcn_update_dpp(0, (int)nn, 0x111, 0xf, 0xf, false));
            nn = max(nn, (unsigned)__builtin_amdgcn_update_dpp(0, (int)nn, 0x112, 0xf, 0xf, false));
            nn = max(nn, (unsigned)__builtin_amdgcn_update_dpp(0, (int)nn, 0x114, 0xf, 0xf, false));
            nn = max(nn, (unsigned)__builtin_amdgcn_update_dpp(0, (int)nn, 0x118, 0xf, 0xf, false));
            nn = max(nn, (unsigned)__builtin_amdgcn_update_dpp(0, (int)nn, 0x142, 0xa, 0xf, false));
            if (r == 31) ((unsigned*)(ws + (isq ? WS_NQ : WS_NK)))[(head + h) * 64 + tile] = nn;
        }
    }
}

__global__ __launch_bounds__(768, 3) void k1_qkv(const u16* __restrict__ x, const u16* __restrict__ wqkv, char* __restrict__ ws) {
    __shared__ __attribute__((aligned(16))) char lds[K1_LDS];
    const unsigned lds0 = (unsigned)(uintptr_t)lds;
    const int b = blockIdx.x, xc = b & 7, i = b >> 3;
    const int mt = (xc >> 1) * 4 + (i & 3), nt = (xc & 1) * 8 + (i >> 2);
    const int lane = threadIdx.x & 63, w = rfl(threadIdx.x >> 6), j = w & 3;
    STAMP_AT(ws, 0, 0);
    if (w < 4) {
        if (b == 0 && w == 0) *(unsigned*)(ws + WS_CNT + lane * 128) = 0u;
        if (nt < 10) k1_compute<0>(lds0, w, lane, mt, nt, ws);
        else if (nt == 10) k1_compute<1>(lds0, w, lane, mt, nt, ws);
        else k1_compute<3>(lds0, w, lane, mt, nt, ws);
        STAMP_AT(ws, 0, 31);
    } else if (w < 8) {
        unsigned xo[4], wof[3];
#pragma unroll
        for (int p = 0; p < 4; ++p) { const int row = 8 * (4 * j + p) + (lane >> 3); xo[p] = row * 1024 + (((lane & 7) ^ ((row >> 1) & 7)) * 16); }
#pragma unroll
        for (int p = 0; p < 3; ++p) { const int off = (3 * j + p) * 1024 + lane * 16; wof[p] = (off / 192) * 3072 + (off % 192); }
        const char* xb = (const char*)x + (size_t)mt * 128 * 1024;
        const char* wb = (const char*)wqkv + nt * 192;
#define K1_ROT ((i >> 2) + 2 * (i & 3))
        const int rot = K1_ROT;
        auto issue = [&](int c, int slot) {
            const unsigned sb = lds0 + slot * K1_SLOT;
            const int kc = (c + rot) & 7;
#pragma unroll
            for (int p = 0; p < 4; ++p) glds16(xb + kc * 128, xo[p], sb + (4 * j + p) * 1024);
#pragma unroll
            for (int p = 0; p < 3; ++p) glds16(wb + (size_t)kc * 64 * 3072, wof[p], sb + 16384 + (3 * j + p) * 1024);
        };
        issue(0, 0); issue(1, 1); issue(2, 2);
        WAITV_BAR(14); issue(3, 3); issue(4, 4);
        WAITV_BAR(21); issue(5, 0);
        WAITV_BAR(21); issue(6, 1);
        WAITV_BAR(21); issue(7, 2);
        WAITV_BAR(21);
        WAITV_BAR(14);
        WAITV_BAR(7);
        WAITV_BAR(0);
        asm volatile("s_barrier" ::: "memory");
    } else {
        const int r = lane & 31, h = lane >> 5;
        float s0 = 0.f, s1 = 0.f, q0 = 0.f, q1 = 0.f;
        u32x4 v[4] = {};
        auto acc2 = [&](int s) {
            float lo[4], hi[4];
#pragma unroll
            for (int e = 0; e < 4; ++e) { lo[e] = bf_lo(v[s][e]); hi[e] = bf_hi(v[s][e]); }
            asm("v_add_f32 %0, %0, %4\n\tv_add_f32 %1, %1, %8\n\tv_fmac_f32 %2, %4, %4\n\tv_fmac_f32 %3, %8, %8\n\t"
                "v_add_f32 %0, %0, %5\n\tv_add_f32 %1, %1, %9\n\tv_fmac_f32 %2, %5, %5\n\tv_fmac_f32 %3, %9, %9\n\t"
                "v_add_f32 %0, %0, %6\n\tv_add_f32 %1, %1, %10\n\tv_fmac_f32 %2, %6, %6\n\tv_fmac_f32 %3, %10, %10\n\t"
                "v_add_f32 %0, %0, %7\n\tv_add_f32 %1, %1, %11\n\tv_fmac_f32 %2, %7, %7\n\tv_fmac_f32 %3, %11, %11"
                : "+v"(s0), "+v"(s1), "+v"(q0), "+v"(q1) : "v"(lo[0]), "v"(lo[1]), "v"(lo[2]), "v"(lo[3]), "v"(hi[0]), "v"(hi[1]), "v"(hi[2]), "v"(hi[3]));
        };
        int slot = 0;
#pragma nounroll
        for (int c = 0; c < 8; ++c) {
            WAITL_BAR();
            const unsigned sb = lds0 + slot * K1_SLOT + (32 * j + r) * 128;
            v[0] = lds_ld128(sb + ((h ^ ((r >> 1) & 7)) * 16)); v[1] = lds_ld128(sb + (((2 + h) ^ ((r >> 1) & 7)) * 16));
            __builtin_amdgcn_sched_barrier(0);
            acc2(2); acc2(3);
            __builtin_amdgcn_sched_barrier(0);
            v[2] = lds_ld128(sb + (((4 + h) ^ ((r >> 1) & 7)) * 16)); v[3] = lds_ld128(sb + (((6 + h) ^ ((r >> 1) & 7)) * 16));
            __builtin_amdgcn_sched_barrier(0);
            acc2(0); acc2(1);
            __builtin_amdgcn_sched_barrier(0);
            slot = (slot == K1_NSLOT - 1) ? 0 : slot + 1;
        }
        acc2(2); acc2(3);
        const float mu = sum_halves(s0 + s1) * (1.f / DM);
        const float rstd = __builtin_amdgcn_rsqf(fmaxf(sum_halves(q0 + q1) * (1.f / DM) - mu * mu, 0.f) + LN_EPS);
        *(LAS float*)(lds0 + K1_ST + j * 256 + lane * 4) = h ? -mu * rstd : rstd;
        WAITL_BAR();
    }
}

constexpr int L_K = 0, L_V = 65536, L_ONE = 131072, L_R = L_ONE + 4096, L_CS = L_R + 24576, L_ST = L_CS + 1024, L_FLAG = L_ST + 1024, L_END = L_FLAG + 64;
#define WB_AUX 16
#define X_POISON 1
#define X_RETILE 1
#define X_WB 1
#define X_CNT 1
#define X_WARM 1
#define PROBE_SLEEP 2
constexpr unsigned SENT = 0xFFFFFFFFu;

typedef __amdgpu_buffer_rsrc_t rsrc_t;
__device__ __forceinline__ u32x4 ld_sc1(rsrc_t rs, unsigned voff, unsigned soff) { return __builtin_amdgcn_raw_buffer_load_b128(rs, voff, soff, 16); }
__device__ __forceinline__ void st_sc1(rsrc_t rs, unsigned voff, unsigned soff, u32x4 v) {
    __builtin_amdgcn_raw_buffer_store_b128(v, rs, voff, soff, 16);
    asm volatile("s_nop 1" :: "v"(v) : "memory");
}
__device__ __forceinline__ unsigned clean(unsigned v) { return min(v, 0xFFFFFFFEu); }
__device__ __forceinline__ unsigned umax4(unsigned m, u32x4 v) { return max(max(m, max(v[0], v[1])), max(v[2], v[3])); }
__device__ __forceinline__ void glds16_sc1(const void* base, unsigned voff, unsigned lds_dst) {
    unsigned keep;
    asm volatile("s_mov_b32 %0, m0\n\ts_mov_b32 m0, %3\n\ts_nop 0\n\tglobal_load_lds_dwordx4 %1, %2 sc1\n\ts_mov_b32 m0, %0"
                 : "=&s"(keep) : "v"(voff), "s"(base), "s"(lds_dst) : "memory");
}
__device__ __forceinline__ float max16(const f32x16& s) {
    float m = fmaxf(s[0], s[1]);
#pragma unroll
    for (int i = 2; i < 16; ++i) m = fmaxf(m, s[i]);
    return m;
}
__device__ __forceinline__ f32x16 splat16(float v) { f32x16 r;
#pragma unroll
    for (int i = 0; i < 16; ++i) r[i] = v;
    return r; }
__device__ __forceinline__ unsigned piece(int idx) { return (unsigned)((idx >> 4) * 32768 + (idx & 15) * 1024); }

static_assert(WS_H == 0x800000 && WS_ST == 0x16c6000, "offsets used inside the asm text");
#define ATT_STAGE_A \
    "s_mov_b32 s88, m0\n\ts_mov_b64 s[86:87], %[kvs]\n\ts_mov_b32 s89, %[kvd]\n\ts_bitcmp1_b32 %[flg], 0\n\ts_cselect_b64 vcc, -1, 0\n\ts_bitcmp1_b32 %[flg], 2\n\t" \
    "s_cselect_b32 s91, 0x1c000, 0\n\tv_mov_b32 v112, -1\n\tv_mov_b32 v113, -1\n\tv_mov_b32 v114, -1\n\tv_mov_b32 v115, -1\n\tv_lshrrev_b32 v116, 2, %[l16]\n\t" \
    "v_lshlrev_b32 v117, 6, %[l16]\n\tv_mov_b32 v93, %[ka]\n\tv_mov_b32 v94, %[va]\n\tv_mov_b32 v95, %[vs]\n\tds_read_b128 v[68:71], v93\n\tds_read_b128 v[72:75], v93 offset:1024\n\t" \
    "ds_read_b128 v[76:79], v94\n\tds_read_b128 v[80:83], v94 offset:512\n\tv_mov_b32 v16, %[negm]\n\tv_mov_b32 v17, %[negm]\n\tv_mov_b32 v18, %[negm]\n\tv_mov_b32 v19, %[negm]\n\t" \
    "v_mov_b32 v20, %[negm]\n\tv_mov_b32 v21, %[negm]\n\tv_mov_b32 v22, %[negm]\n\tv_mov_b32 v23, %[negm]\n\tv_mov_b32 v24, %[negm]\n\tv_mov_b32 v25, %[negm]\n\t" \
    "v_mov_b32 v26, %[negm]\n\tv_mov_b32 v27, %[negm]\n\tv_mov_b32 v28, %[negm]\n\tv_mov_b32 v29, %[negm]\n\tv_mov_b32 v30, %[negm]\n\tv_mov_b32 v31, %[negm]\n\t" \
    "v_mov_b32 v92, 0xdddddddd\n\ts_waitcnt lgkmcnt(3)\n\tv_mfma_f32_32x32x16_bf16 v[32:47], v[68:71], v[64:67], v[16:31]\n\tv_mov_b64 v[0:1], 0\n\tv_mov_b64 v[2:3], 0\n\t" \
    "v_mov_b64 v[4:5], 0\n\tv_mov_b64 v[6:7], 0\n\tv_mov_b64 v[8:9], 0\n\tv_mov_b64 v[10:11], 0\n\tv_mov_b64 v[12:13], 0\n\tv_mov_b64 v[14:15], 0\n\tv_mov_b64 v[56:57], 0\n\t" \
    "v_mov_b64 v[58:59], 0\n\tv_mov_b64 v[60:61], 0\n\tv_mov_b64 v[62:63], 0\n\tv_mov_b64 v[88:89], 0\n\tv_mov_b64 v[90:91], 0\n\ts_waitcnt lgkmcnt(0)\n\tv_exp_f32 v32, v32\n\t" \
    "v_exp_f32 v33, v33\n\tv_smfmac_f32_32x32x32_bf16 v[0:15], v[88:91], v[56:63], v92\n\ts_mov_b32 m0, s89\n\ts_add_u32 s89, s89, 0x1000\n\tglobal_load_lds_dwordx4 %[l16], s[86:87]\n\t" \
    "global_load_lds_dwordx4 %[l16], s[86:87] offset:2048\n\ts_add_u32 s86, s86, 0x1000\n\ts_addc_u32 s87, s87, 0\n\tds_read_b128 v[68:71], v93 offset:2048\n\t" \
    "ds_read_b128 v[84:87], v94 offset:1024\n\tds_read_b128 v[88:91], v94 offset:1536\n\tv_exp_f32 v34, v34\n\tv_exp_f32 v35, v35\n\tv_exp_f32 v36, v36\n\ts_waitcnt lgkmcnt(3)\n\t" \
    "v_mfma_f32_32x32x16_bf16 v[48:63], v[72:75], v[64:67], v[16:31]\n\tv_exp_f32 v37, v37\n\tv_exp_f32 v38, v38\n\tv_exp_f32 v39, v39\n\tv_exp_f32 v40, v40\n\tv_exp_f32 v41, v41\n\t" \
    "v_exp_f32 v42, v42\n\tv_smfmac_f32_32x32x32_bf16 v[0:15], v[76:79], v[32:39], v92\n\tv_exp_f32 v43, v43\n\tv_exp_f32 v44, v44\n\tv_exp_f32 v45, v45\n\tv_exp_f32 v46, v46\n\t" \
    "v_exp_f32 v47, v47\n\tv_exp_f32 v48, v48\n\tv_exp_f32 v49, v49\n\tv_smfmac_f32_32x32x32_bf16 v[0:15], v[80:83], v[40:47], v92\n\ts_mov_b32 m0, s89\n\ts_add_u32 s89, s89, 0x1000\n\t" \
    "global_load_lds_dwordx4 %[l16], s[86:87]\n\tglobal_load_lds_dwordx4 %[l16], s[86:87] offset:2048\n\ts_add_u32 s86, s86, 0x1000\n\ts_addc_u32 s87, s87, 0\n\t" \
    "ds_read_b128 v[72:75], v93 offset:3072\n\tds_read_b128 v[76:79], v94 offset:2048\n\tds_read_b128 v[80:83], v94 offset:2560\n\tv_exp_f32 v50, v50\n\tv_exp_f32 v51, v51\n\t" \
    "v_exp_f32 v52, v52\n\ts_waitcnt lgkmcnt(3)\n\tv_mfma_f32_32x32x16_bf16 v[32:47], v[68:71], v[64:67], v[16:31]\n\tv_exp_f32 v53, v53\n\tv_exp_f32 v54, v54\n\tv_exp_f32 v55, v55\n\t" \
    "v_exp_f32 v56, v56\n\tv_exp_f32 v57, v57\n\tv_exp_f32 v58, v58\n\tv_smfmac_f32_32x32x32_bf16 v[0:15], v[84:87], v[48:55], v92\n\tv_exp_f32 v59, v59\n\tv_exp_f32 v60, v60\n\t" \
    "v_exp_f32 v61, v61\n\tv_exp_f32 v62, v62\n\tv_exp_f32 v63, v63\n\tv_exp_f32 v32, v32\n\tv_exp_f32 v33, v33\n\tv_smfmac_f32_32x32x32_bf16 v[0:15], v[88:91], v[56:63], v92\n\t" \
    "s_mov_b32 m0, s89\n\ts_add_u32 s89, s89, 0x1000\n\tglobal_load_lds_dwordx4 %[l16], s[86:87]\n\tglobal_load_lds_dwordx4 %[l16], s[86:87] offset:2048\n\t" \
    "s_add_u32 s86, s86, 0x1000\n\ts_addc_u32 s87, s87, 0\n\tds_read_b128 v[68:71], v93 offset:4096\n\tds_read_b128 v[84:87], v94 offset:3072\n\t" \
    "ds_read_b128 v[88:91], v94 offset:3584\n\tv_exp_f32 v34, v34\n\tv_exp_f32 v35, v35\n\tv_exp_f32 v36, v36\n\ts_waitcnt lgkmcnt(3)\n\t" \
    "v_mfma_f32_32x32x16_bf16 v[48:63], v[72:75], v[64:67], v[16:31]\n\tv_exp_f32 v37, v37\n\tv_exp_f32 v38, v38\n\tv_exp_f32 v39, v39\n\tv_exp_f32 v40, v40\n\tv_exp_f32 v41, v41\n\t" \
    "v_exp_f32 v42, v42\n\tv_smfmac_f32_32x32x32_bf16 v[0:15], v[76:79], v[32:39], v92\n\tv_exp_f32 v43, v43\n\tv_exp_f32 v44, v44\n\tv_exp_f32 v45, v45\n\tv_exp_f32 v46, v46\n\t" \
    "v_exp_f32 v47, v47\n\tv_exp_f32 v48, v48\n\tv_exp_f32 v49, v49\n\tv_smfmac_f32_32x32x32_bf16 v[0:15], v[80:83], v[40:47], v92\n\ts_mov_b32 m0, s89\n\ts_add_u32 s89, s89, 0x1000\n\t" \
    "global_load_lds_dwordx4 %[l16], s[86:87]\n\tglobal_load_lds_dwordx4 %[l16], s[86:87] offset:2048\n\ts_add_u32 s86, s86, 0x1000\n\ts_addc_u32 s87, s87, 0\n\t" \
    "v_add_u32 v94, v94, v95\n\tds_read_b128 v[72:75], v93 offset:5120\n\tds_read_b128 v[76:79], v94 offset:0\n\tds_read_b128 v[80:83], v94 offset:512\n\tv_exp_f32 v50, v50\n\t" \
    "v_exp_f32 v51, v51\n\tv_exp_f32 v52, v52\n\ts_waitcnt lgkmcnt(3)\n\tv_mfma_f32_32x32x16_bf16 v[32:47], v[68:71], v[64:67], v[16:31]\n\tv_exp_f32 v53, v53\n\tv_exp_f32 v54, v54\n\t" \
    "v_exp_f32 v55, v55\n\tv_exp_f32 v56, v56\n\tv_exp_f32 v57, v57\n\tv_exp_f32 v58, v58\n\tv_smfmac_f32_32x32x32_bf16 v[0:15], v[84:87], v[48:55], v92\n\tv_exp_f32 v59, v59\n\t" \
    "v_exp_f32 v60, v60\n\tv_exp_f32 v61, v61\n\tv_exp_f32 v62, v62\n\tv_exp_f32 v63, v63\n\tv_add_u32 v93, 0x1000, v93\n\ts_waitcnt vmcnt(9)\n\ts_barrier\n\ts_setprio 3\n\t" \
    "s_mov_b32 s89, %[wld]\n\tv_exp_f32 v32, v32\n\tv_exp_f32 v33, v33\n\tv_smfmac_f32_32x32x32_bf16 v[0:15], v[88:91], v[56:63], v92\n\ts_cbranch_vccz 1f\n\ts_mov_b32 m0, s89\n\t" \
    "s_add_u32 s89, s89, 0x400\n\tglobal_load_lds_dwordx4 %[wv0], %[w0]\n\t1:\n\tds_read_b128 v[68:71], v93 offset:2048\n\tds_read_b128 v[84:87], v94 offset:1024\n\t" \
    "ds_read_b128 v[88:91], v94 offset:1536\n\tv_exp_f32 v34, v34\n\tv_exp_f32 v35, v35\n\tv_exp_f32 v36, v36\n\ts_waitcnt lgkmcnt(3)\n\t" \
    "v_mfma_f32_32x32x16_bf16 v[48:63], v[72:75], v[64:67], v[16:31]\n\tv_exp_f32 v37, v37\n\tv_exp_f32 v38, v38\n\tv_exp_f32 v39, v39\n\tv_exp_f32 v40, v40\n\tv_exp_f32 v41, v41\n\t" \
    "v_exp_f32 v42, v42\n\tv_smfmac_f32_32x32x32_bf16 v[0:15], v[76:79], v[32:39], v92\n\tv_exp_f32 v43, v43\n\tv_exp_f32 v44, v44\n\tv_exp_f32 v45, v45\n\tv_exp_f32 v46, v46\n\t" \
    "v_exp_f32 v47, v47\n\tv_exp_f32 v48, v48\n\tv_exp_f32 v49, v49\n\tv_smfmac_f32_32x32x32_bf16 v[0:15], v[80:83], v[40:47], v92\n\ts_cbranch_vccz 2f\n\ts_mov_b32 m0, s89\n\t" \
    "s_add_u32 s89, s89, 0x400\n\tglobal_load_lds_dwordx4 %[wv1], %[w0]\n\t2:\n\tds_read_b128 v[72:75], v93 offset:3072\n\tds_read_b128 v[76:79], v94 offset:2048\n\t" \
    "ds_read_b128 v[80:83], v94 offset:2560\n\tv_exp_f32 v50, v50\n\tv_exp_f32 v51, v51\n\tv_exp_f32 v52, v52\n\ts_waitcnt lgkmcnt(3)\n\t" \
    "v_mfma_f32_32x32x16_bf16 v[32:47], v[68:71], v[64:67], v[16:31]\n\tv_exp_f32 v53, v53\n\tv_exp_f32 v54, v54\n\tv_exp_f32 v55, v55\n\tv_exp_f32 v56, v56\n\tv_exp_f32 v57, v57\n\t" \
    "v_exp_f32 v58, v58\n\tv_smfmac_f32_32x32x32_bf16 v[0:15], v[84:87], v[48:55], v92\n\tv_exp_f32 v59, v59\n\tv_exp_f32 v60, v60\n\tv_exp_f32 v61, v61\n\tv_exp_f32 v62, v62\n\t" \
    "v_exp_f32 v63, v63\n\tv_exp_f32 v32, v32\n\tv_exp_f32 v33, v33\n\tv_smfmac_f32_32x32x32_bf16 v[0:15], v[88:91], v[56:63], v92\n\ts_cbranch_vccz 3f\n\ts_mov_b32 m0, s89\n\t" \
    "s_add_u32 s89, s89, 0x400\n\tglobal_load_lds_dwordx4 %[wv0], %[w2]\n\t3:\n\tds_read_b128 v[68:71], v93 offset:4096\n\tds_read_b128 v[84:87], v94 offset:3072\n\t" \
    "ds_read_b128 v[88:91], v94 offset:3584\n\tv_exp_f32 v34, v34\n\tv_exp_f32 v35, v35\n\tv_exp_f32 v36, v36\n\ts_waitcnt lgkmcnt(3)\n\t" \
    "v_mfma_f32_32x32x16_bf16 v[48:63], v[72:75], v[64:67], v[16:31]\n\tv_exp_f32 v37, v37\n\tv_exp_f32 v38, v38\n\tv_exp_f32 v39, v39\n\tv_exp_f32 v40, v40\n\tv_exp_f32 v41, v41\n\t" \
    "v_exp_f32 v42, v42\n\tv_smfmac_f32_32x32x32_bf16 v[0:15], v[76:79], v[32:39], v92\n\tv_exp_f32 v43, v43\n\tv_exp_f32 v44, v44\n\tv_exp_f32 v45, v45\n\tv_exp_f32 v46, v46\n\t" \
    "v_exp_f32 v47, v47\n\tv_exp_f32 v48, v48\n\tv_exp_f32 v49, v49\n\tv_smfmac_f32_32x32x32_bf16 v[0:15], v[80:83], v[40:47], v92\n\ts_cbranch_vccz 4f\n\ts_mov_b32 m0, s89\n\t" \
    "s_add_u32 s89, s89, 0x400\n\tglobal_load_lds_dwordx4 %[wv1], %[w2]\n\t4:\n\tv_add_u32 v94, v94, v95\n\tds_read_b128 v[72:75], v93 offset:5120\n\t" \
    "ds_read_b128 v[76:79], v94 offset:0\n\tds_read_b128 v[80:83], v94 offset:512\n\tv_exp_f32 v50, v50\n\tv_exp_f32 v51, v51\n\tv_exp_f32 v52, v52\n\ts_waitcnt lgkmcnt(3)\n\t" \
    "v_mfma_f32_32x32x16_bf16 v[32:47], v[68:71], v[64:67], v[16:31]\n\tv_exp_f32 v53, v53\n\tv_exp_f32 v54, v54\n\tv_exp_f32 v55, v55\n\tv_exp_f32 v56, v56\n\tv_exp_f32 v57, v57\n\t" \
    "v_exp_f32 v58, v58\n\tv_smfmac_f32_32x32x32_bf16 v[0:15], v[84:87], v[48:55], v92\n\tv_exp_f32 v59, v59\n\tv_exp_f32 v60, v60\n\tv_exp_f32 v61, v61\n\tv_exp_f32 v62, v62\n\t" \
    "v_exp_f32 v63, v63\n\tv_add_u32 v93, 0x1000, v93\n\ts_setprio 2\n\ts_mov_b32 s89, %[pa]\n\ts_sub_u32 s90, %[ph], 0x800000\n\ts_lshr_b32 s90, s90, 2\n\t" \
    "s_add_u32 s90, s90, 0x16c6000\n\tv_exp_f32 v32, v32\n\tv_exp_f32 v33, v33\n\tv_smfmac_f32_32x32x32_bf16 v[0:15], v[88:91], v[56:63], v92\n\t" \
    "buffer_store_dwordx4 v[112:115], %[l16], %[rs], %[po] offen sc1\n\tds_read_b128 v[68:71], v93 offset:2048\n\tds_read_b128 v[84:87], v94 offset:1024\n\t" \
    "ds_read_b128 v[88:91], v94 offset:1536\n\tv_exp_f32 v34, v34\n\tv_exp_f32 v35, v35\n\tv_exp_f32 v36, v36\n\ts_waitcnt lgkmcnt(3)\n\t" \
    "v_mfma_f32_32x32x16_bf16 v[48:63], v[72:75], v[64:67], v[16:31]\n\tv_exp_f32 v37, v37\n\tv_exp_f32 v38, v38\n\tv_exp_f32 v39, v39\n\tv_exp_f32 v40, v40\n\tv_exp_f32 v41, v41\n\t" \
    "v_exp_f32 v42, v42\n\tv_smfmac_f32_32x32x32_bf16 v[0:15], v[76:79], v[32:39], v92\n\tv_exp_f32 v43, v43\n\tv_exp_f32 v44, v44\n\tv_exp_f32 v45, v45\n\tv_exp_f32 v46, v46\n\t" \
    "v_exp_f32 v47, v47\n\tv_exp_f32 v48, v48\n\tv_exp_f32 v49, v49\n\tv_smfmac_f32_32x32x32_bf16 v[0:15], v[80:83], v[40:47], v92\n\t" \
    "buffer_store_dwordx4 v[112:115], %[l16], %[rs], %[ph] offen sc1\n\tds_read_b128 v[72:75], v93 offset:3072\n\tds_read_b128 v[76:79], v94 offset:2048\n\t" \
    "ds_read_b128 v[80:83], v94 offset:2560\n\tv_exp_f32 v50, v50\n\tv_exp_f32 v51, v51\n\tv_exp_f32 v52, v52\n\ts_waitcnt lgkmcnt(3)\n\t" \
    "v_mfma_f32_32x32x16_bf16 v[32:47], v[68:71], v[64:67], v[16:31]\n\tv_exp_f32 v53, v53\n\tv_exp_f32 v54, v54\n\tv_exp_f32 v55, v55\n\tv_exp_f32 v56, v56\n\tv_exp_f32 v57, v57\n\t" \
    "v_exp_f32 v58, v58\n\tv_smfmac_f32_32x32x32_bf16 v[0:15], v[84:87], v[48:55], v92\n\tv_exp_f32 v59, v59\n\tv_exp_f32 v60, v60\n\tv_exp_f32 v61, v61\n\tv_exp_f32 v62, v62\n\t" \
    "v_exp_f32 v63, v63\n\tv_exp_f32 v32, v32\n\tv_exp_f32 v33, v33\n\tv_smfmac_f32_32x32x32_bf16 v[0:15], v[88:91], v[56:63], v92\n\t" \
    "buffer_store_dword v112, v116, %[rs], s90 offen sc1\n\tds_read_b128 v[68:71], v93 offset:4096\n\tds_read_b128 v[84:87], v94 offset:3072\n\t" \
    "ds_read_b128 v[88:91], v94 offset:3584\n\tv_exp_f32 v34, v34\n\tv_exp_f32 v35, v35\n\tv_exp_f32 v36, v36\n\ts_waitcnt lgkmcnt(3)\n\t" \
    "v_mfma_f32_32x32x16_bf16 v[48:63], v[72:75], v[64:67], v[16:31]\n\tv_exp_f32 v37, v37\n\tv_exp_f32 v38, v38\n\tv_exp_f32 v39, v39\n\tv_exp_f32 v40, v40\n\tv_exp_f32 v41, v41\n\t" \
    "v_exp_f32 v42, v42\n\tv_smfmac_f32_32x32x32_bf16 v[0:15], v[76:79], v[32:39], v92\n\tv_exp_f32 v43, v43\n\tv_exp_f32 v44, v44\n\tv_exp_f32 v45, v45\n\tv_exp_f32 v46, v46\n\t" \
    "v_exp_f32 v47, v47\n\tv_exp_f32 v48, v48\n\tv_exp_f32 v49, v49\n\tv_smfmac_f32_32x32x32_bf16 v[0:15], v[80:83], v[40:47], v92\n\t" \
    "buffer_store_dwordx4 v[112:115], %[l16], %[rs], s89 offen sc1\n\ts_add_u32 s89, s89, s91\n\tv_add_u32 v94, v94, v95\n\tds_read_b128 v[72:75], v93 offset:5120\n\t" \
    "ds_read_b128 v[76:79], v94 offset:0\n\tds_read_b128 v[80:83], v94 offset:512\n\tv_exp_f32 v50, v50\n\tv_exp_f32 v51, v51\n\tv_exp_f32 v52, v52\n\ts_waitcnt lgkmcnt(3)\n\t" \
    "v_mfma_f32_32x32x16_bf16 v[32:47], v[68:71], v[64:67], v[16:31]\n\tv_exp_f32 v53, v53\n\tv_exp_f32 v54, v54\n\tv_exp_f32 v55, v55\n\tv_exp_f32 v56, v56\n\tv_exp_f32 v57, v57\n\t" \
    "v_exp_f32 v58, v58\n\tv_smfmac_f32_32x32x32_bf16 v[0:15], v[84:87], v[48:55], v92\n\tv_exp_f32 v59, v59\n\tv_exp_f32 v60, v60\n\tv_exp_f32 v61, v61\n\tv_exp_f32 v62, v62\n\t" \
    "v_exp_f32 v63, v63\n\tv_add_u32 v93, 0x1000, v93\n\ts_waitcnt vmcnt(8)\n\ts_cbranch_vccnz 5f\n\ts_waitcnt vmcnt(4)\n\t5:\n\ts_barrier\n\ts_setprio 3\n\tv_exp_f32 v32, v32\n\t" \
    "v_exp_f32 v33, v33\n\tv_smfmac_f32_32x32x32_bf16 v[0:15], v[88:91], v[56:63], v92\n\tbuffer_store_dwordx4 v[112:115], %[l16], %[rs], s89 offen sc1\n\ts_mul_i32 s90, s91, 3\n\t" \
    "s_add_u32 s89, s89, s90\n\tds_read_b128 v[68:71], v93 offset:2048\n\tds_read_b128 v[84:87], v94 offset:1024\n\tds_read_b128 v[88:91], v94 offset:1536\n\tv_exp_f32 v34, v34\n\t" \
    "v_exp_f32 v35, v35\n\tv_exp_f32 v36, v36\n\ts_waitcnt lgkmcnt(3)\n\tv_mfma_f32_32x32x16_bf16 v[48:63], v[72:75], v[64:67], v[16:31]\n\tv_exp_f32 v37, v37\n\tv_exp_f32 v38, v38\n\t" \
    "v_exp_f32 v39, v39\n\tv_exp_f32 v40, v40\n\tv_exp_f32 v41, v41\n\tv_exp_f32 v42, v42\n\tv_smfmac_f32_32x32x32_bf16 v[0:15], v[76:79], v[32:39], v92\n\tv_exp_f32 v43, v43\n\t" \
    "v_exp_f32 v44, v44\n\tv_exp_f32 v45, v45\n\tv_exp_f32 v46, v46\n\tv_exp_f32 v47, v47\n\tv_exp_f32 v48, v48\n\tv_exp_f32 v49, v49\n\t" \
    "v_smfmac_f32_32x32x32_bf16 v[0:15], v[80:83], v[40:47], v92\n\tbuffer_store_dwordx4 v[112:115], %[l16], %[rs], s89 offen sc1\n\ts_add_u32 s89, s89, s91\n\t" \
    "ds_read_b128 v[72:75], v93 offset:3072\n\tds_read_b128 v[76:79], v94 offset:2048\n\tds_read_b128 v[80:83], v94 offset:2560\n\tv_exp_f32 v50, v50\n\tv_exp_f32 v51, v51\n\t" \
    "v_exp_f32 v52, v52\n\ts_waitcnt lgkmcnt(3)\n\tv_mfma_f32_32x32x16_bf16 v[32:47], v[68:71], v[64:67], v[16:31]\n\tv_exp_f32 v53, v53\n\tv_exp_f32 v54, v54\n\tv_exp_f32 v55, v55\n\t" \
    "v_exp_f32 v56, v56\n\tv_exp_f32 v57, v57\n\tv_exp_f32 v58, v58\n\tv_smfmac_f32_32x32x32_bf16 v[0:15], v[84:87], v[48:55], v92\n\tv_exp_f32 v59, v59\n\tv_exp_f32 v60, v60\n\t" \
    "v_exp_f32 v61, v61\n\tv_exp_f32 v62, v62\n\tv_exp_f32 v63, v63\n\tv_exp_f32 v32, v32\n\tv_exp_f32 v33, v33\n\tv_smfmac_f32_32x32x32_bf16 v[0:15], v[88:91], v[56:63], v92\n\t" \
    "buffer_store_dwordx4 v[112:115], %[l16], %[rs], s89 offen sc1\n\tds_read_b128 v[68:71], v93 offset:4096\n\tds_read_b128 v[84:87], v94 offset:3072\n\t" \
    "ds_read_b128 v[88:91], v94 offset:3584\n\tv_exp_f32 v34, v34\n\tv_exp_f32 v35, v35\n\tv_exp_f32 v36, v36\n\ts_waitcnt lgkmcnt(3)\n\t" \
    "v_mfma_f32_32x32x16_bf16 v[48:63], v[72:75], v[64:67], v[16:31]\n\tv_exp_f32 v37, v37\n\tv_exp_f32 v38, v38\n\tv_exp_f32 v39, v39\n\tv_exp_f32 v40, v40\n\tv_exp_f32 v41, v41\n\t" \
    "v_exp_f32 v42, v42\n\tv_smfmac_f32_32x32x32_bf16 v[0:15], v[76:79], v[32:39], v92\n\tv_exp_f32 v43, v43\n\tv_exp_f32 v44, v44\n\tv_exp_f32 v45, v45\n\tv_exp_f32 v46, v46\n\t" \
    "v_exp_f32 v47, v47\n\tv_exp_f32 v48, v48\n\tv_exp_f32 v49, v49\n\tv_smfmac_f32_32x32x32_bf16 v[0:15], v[80:83], v[40:47], v92\n\ts_cbranch_vccz 6f\n\ts_waitcnt vmcnt(7)\n\t" \
    "ds_read_b64_tr_b16 v[96:97], %[t0] offset:0\n\tds_read_b64_tr_b16 v[98:99], %[t0] offset:512\n\tds_read_b64_tr_b16 v[100:101], %[t0] offset:1024\n\t" \
    "ds_read_b64_tr_b16 v[102:103], %[t0] offset:1536\n\t6:\n\tv_add_u32 v94, v94, v95\n\tds_read_b128 v[72:75], v93 offset:5120\n\tds_read_b128 v[76:79], v94 offset:0\n\t" \
    "ds_read_b128 v[80:83], v94 offset:512\n\tv_exp_f32 v50, v50\n\tv_exp_f32 v51, v51\n\tv_exp_f32 v52, v52\n\ts_waitcnt lgkmcnt(3)\n\t" \
    "v_mfma_f32_32x32x16_bf16 v[32:47], v[68:71], v[64:67], v[16:31]\n\tv_exp_f32 v53, v53\n\tv_exp_f32 v54, v54\n\tv_exp_f32 v55, v55\n\tv_exp_f32 v56, v56\n\tv_exp_f32 v57, v57\n\t" \
    "v_exp_f32 v58, v58\n\tv_smfmac_f32_32x32x32_bf16 v[0:15], v[84:87], v[48:55], v92\n\tv_exp_f32 v59, v59\n\tv_exp_f32 v60, v60\n\tv_exp_f32 v61, v61\n\tv_exp_f32 v62, v62\n\t" \
    "v_exp_f32 v63, v63\n\tv_add_u32 v93, 0x1000, v93\n\tv_exp_f32 v32, v32\n\tv_exp_f32 v33, v33\n\tv_smfmac_f32_32x32x32_bf16 v[0:15], v[88:91], v[56:63], v92\n\ts_cbranch_vccz 7f\n\t" \
    "ds_read_b64_tr_b16 v[104:105], %[t2] offset:0\n\tds_read_b64_tr_b16 v[106:107], %[t2] offset:512\n\tds_read_b64_tr_b16 v[108:109], %[t2] offset:1024\n\t" \
    "ds_read_b64_tr_b16 v[110:111], %[t2] offset:1536\n\tbuffer_store_dwordx4 v[96:99], %[l16], %[rs], %[d0] offen offset:0 sc1\n\t7:\n\tds_read_b128 v[68:71], v93 offset:2048\n\t" \
    "ds_read_b128 v[84:87], v94 offset:1024\n\tds_read_b128 v[88:91], v94 offset:1536\n\tv_exp_f32 v34, v34\n\tv_exp_f32 v35, v35\n\tv_exp_f32 v36, v36\n\ts_waitcnt lgkmcnt(3)\n\t" \
    "v_mfma_f32_32x32x16_bf16 v[48:63], v[72:75], v[64:67], v[16:31]\n\tv_exp_f32 v37, v37\n\tv_exp_f32 v38, v38\n\tv_exp_f32 v39, v39\n\tv_exp_f32 v40, v40\n\tv_exp_f32 v41, v41\n\t" \
    "v_exp_f32 v42, v42\n\tv_smfmac_f32_32x32x32_bf16 v[0:15], v[76:79], v[32:39], v92\n\tv_exp_f32 v43, v43\n\tv_exp_f32 v44, v44\n\tv_exp_f32 v45, v45\n\tv_exp_f32 v46, v46\n\t" \
    "v_exp_f32 v47, v47\n\tv_exp_f32 v48, v48\n\tv_exp_f32 v49, v49\n\tv_smfmac_f32_32x32x32_bf16 v[0:15], v[80:83], v[40:47], v92\n\ts_cbranch_vccz 8f\n\t" \
    "buffer_store_dwordx4 v[100:103], %[l16], %[rs], %[d0] offen offset:1024 sc1\n\tbuffer_store_dwordx4 v[104:107], %[l16], %[rs], %[d2] offen offset:0 sc1\n\t8:\n\t" \
    "ds_read_b128 v[72:75], v93 offset:3072\n\tds_read_b128 v[76:79], v94 offset:2048\n\tds_read_b128 v[80:83], v94 offset:2560\n\tv_exp_f32 v50, v50\n\tv_exp_f32 v51, v51\n\t" \
    "v_exp_f32 v52, v52\n\ts_waitcnt lgkmcnt(3)\n\tv_mfma_f32_32x32x16_bf16 v[32:47], v[68:71], v[64:67], v[16:31]\n\tv_exp_f32 v53, v53\n\tv_exp_f32 v54, v54\n\tv_exp_f32 v55, v55\n\t" \
    "v_exp_f32 v56, v56\n\tv_exp_f32 v57, v57\n\tv_exp_f32 v58, v58\n\tv_smfmac_f32_32x32x32_bf16 v[0:15], v[84:87], v[48:55], v92\n\tv_exp_f32 v59, v59\n\tv_exp_f32 v60, v60\n\t" \
    "v_exp_f32 v61, v61\n\tv_exp_f32 v62, v62\n\tv_exp_f32 v63, v63\n\tv_exp_f32 v32, v32\n\tv_exp_f32 v33, v33\n\tv_smfmac_f32_32x32x32_bf16 v[0:15], v[88:91], v[56:63], v92\n\t" \
    "s_cbranch_vccz 9f\n\tbuffer_store_dwordx4 v[108:111], %[l16], %[rs], %[d2] offen offset:1024 sc1\n\t9:\n\tds_read_b128 v[68:71], v93 offset:4096\n\t" \
    "ds_read_b128 v[84:87], v94 offset:3072\n\tds_read_b128 v[88:91], v94 offset:3584\n\tv_exp_f32 v34, v34\n\tv_exp_f32 v35, v35\n\tv_exp_f32 v36, v36\n\ts_waitcnt lgkmcnt(3)\n\t" \
    "v_mfma_f32_32x32x16_bf16 v[48:63], v[72:75], v[64:67], v[16:31]\n\tv_exp_f32 v37, v37\n\tv_exp_f32 v38, v38\n\tv_exp_f32 v39, v39\n\tv_exp_f32 v40, v40\n\tv_exp_f32 v41, v41\n\t" \
    "v_exp_f32 v42, v42\n\tv_smfmac_f32_32x32x32_bf16 v[0:15], v[76:79], v[32:39], v92\n\tv_exp_f32 v43, v43\n\tv_exp_f32 v44, v44\n\tv_exp_f32 v45, v45\n\tv_exp_f32 v46, v46\n\t" \
    "v_exp_f32 v47, v47\n\ts_setprio 2\n\tv_exp_f32 v48, v48\n\tv_exp_f32 v49, v49\n\tv_smfmac_f32_32x32x32_bf16 v[0:15], v[80:83], v[40:47], v92\n\tv_add_u32 v94, v94, v95\n\t" \
    "ds_read_b128 v[72:75], v93 offset:5120\n\tds_read_b128 v[76:79], v94 offset:0\n\tds_read_b128 v[80:83], v94 offset:512\n\tv_exp_f32 v50, v50\n\tv_exp_f32 v51, v51\n\t" \
    "v_exp_f32 v52, v52\n\ts_waitcnt lgkmcnt(3)\n\tv_mfma_f32_32x32x16_bf16 v[32:47], v[68:71], v[64:67], v[16:31]\n\tv_exp_f32 v53, v53\n\tv_exp_f32 v54, v54\n\tv_exp_f32 v55, v55\n\t" \
    "v_exp_f32 v56, v56\n\tv_exp_f32 v57, v57\n\tv_exp_f32 v58, v58\n\tv_smfmac_f32_32x32x32_bf16 v[0:15], v[84:87], v[48:55], v92\n\tv_exp_f32 v59, v59\n\tv_exp_f32 v60, v60\n\t" \
    "v_exp_f32 v61, v61\n\tv_exp_f32 v62, v62\n\tv_exp_f32 v63, v63\n\tv_add_u32 v93, 0x1000, v93\n\tv_exp_f32 v32, v32\n\tv_exp_f32 v33, v33\n\t" \
    "v_smfmac_f32_32x32x32_bf16 v[0:15], v[88:91], v[56:63], v92\n\tds_read_b128 v[68:71], v93 offset:2048\n\tds_read_b128 v[84:87], v94 offset:1024\n\t" \
    "ds_read_b128 v[88:91], v94 offset:1536\n\tv_exp_f32 v34, v34\n\tv_exp_f32 v35, v35\n\tv_exp_f32 v36, v36\n\ts_waitcnt lgkmcnt(3)\n\t" \
    "v_mfma_f32_32x32x16_bf16 v[48:63], v[72:75], v[64:67], v[16:31]\n\tv_exp_f32 v37, v37\n\tv_exp_f32 v38, v38\n\tv_exp_f32 v39, v39\n\tv_exp_f32 v40, v40\n\tv_exp_f32 v41, v41\n\t" \
    "v_exp_f32 v42, v42\n\tv_smfmac_f32_32x32x32_bf16 v[0:15], v[76:79], v[32:39], v92\n\tv_exp_f32 v43, v43\n\tv_exp_f32 v44, v44\n\tv_exp_f32 v45, v45\n\tv_exp_f32 v46, v46\n\t" \
    "v_exp_f32 v47, v47\n\tv_exp_f32 v48, v48\n\tv_exp_f32 v49, v49\n\tv_smfmac_f32_32x32x32_bf16 v[0:15], v[80:83], v[40:47], v92\n\tds_read_b128 v[72:75], v93 offset:3072\n\t" \
    "ds_read_b128 v[76:79], v94 offset:2048\n\tds_read_b128 v[80:83], v94 offset:2560\n\tv_exp_f32 v50, v50\n\tv_exp_f32 v51, v51\n\tv_exp_f32 v52, v52\n\ts_waitcnt lgkmcnt(3)\n\t" \
    "v_mfma_f32_32x32x16_bf16 v[32:47], v[68:71], v[64:67], v[16:31]\n\tv_exp_f32 v53, v53\n\tv_exp_f32 v54, v54\n\tv_exp_f32 v55, v55\n\tv_exp_f32 v56, v56\n\tv_exp_f32 v57, v57\n\t" \
    "v_exp_f32 v58, v58\n\tv_smfmac_f32_32x32x32_bf16 v[0:15], v[84:87], v[48:55], v92\n\tv_exp_f32 v59, v59\n\tv_exp_f32 v60, v60\n\tv_exp_f32 v61, v61\n\tv_exp_f32 v62, v62\n\t" \
    "v_exp_f32 v63, v63\n\ts_setprio 1\n\tv_exp_f32 v32, v32\n\tv_exp_f32 v33, v33\n\tv_smfmac_f32_32x32x32_bf16 v[0:15], v[88:91], v[56:63], v92\n\t" \
    "ds_read_b128 v[68:71], v93 offset:4096\n\tds_read_b128 v[84:87], v94 offset:3072\n\tds_read_b128 v[88:91], v94 offset:3584\n\tv_exp_f32 v34, v34\n\tv_exp_f32 v35, v35\n\t" \
    "v_exp_f32 v36, v36\n\ts_waitcnt lgkmcnt(3)\n\tv_mfma_f32_32x32x16_bf16 v[48:63], v[72:75], v[64:67], v[16:31]\n\tv_exp_f32 v37, v37\n\tv_exp_f32 v38, v38\n\tv_exp_f32 v39, v39\n\t" \
    "v_exp_f32 v40, v40\n\tv_exp_f32 v41, v41\n\tv_exp_f32 v42, v42\n\tv_smfmac_f32_32x32x32_bf16 v[0:15], v[76:79], v[32:39], v92\n\tv_exp_f32 v43, v43\n\tv_exp_f32 v44, v44\n\t" \
    "v_exp_f32 v45, v45\n\tv_exp_f32 v46, v46\n\tv_exp_f32 v47, v47\n\tv_exp_f32 v48, v48\n\tv_exp_f32 v49, v49\n\tv_smfmac_f32_32x32x32_bf16 v[0:15], v[80:83], v[40:47], v92\n\t" \
    "v_add_u32 v94, v94, v95\n\tds_read_b128 v[72:75], v93 offset:5120\n\tds_read_b128 v[76:79], v94 offset:0\n\tds_read_b128 v[80:83], v94 offset:512\n\tv_exp_f32 v50, v50\n\t" \
    "v_exp_f32 v51, v51\n\tv_exp_f32 v52, v52\n\ts_waitcnt lgkmcnt(3)\n\tv_mfma_f32_32x32x16_bf16 v[32:47], v[68:71], v[64:67], v[16:31]\n\tv_exp_f32 v53, v53\n\tv_exp_f32 v54, v54\n\t" \
    "v_exp_f32 v55, v55\n\tv_exp_f32 v56, v56\n\tv_exp_f32 v57, v57\n\tv_exp_f32 v58, v58\n\tv_smfmac_f32_32x32x32_bf16 v[0:15], v[84:87], v[48:55], v92\n\tv_exp_f32 v59, v59\n\t" \
    "v_exp_f32 v60, v60\n\tv_exp_f32 v61, v61\n\tv_exp_f32 v62, v62\n\tv_exp_f32 v63, v63\n\tv_add_u32 v93, 0x1000, v93\n\ts_waitcnt vmcnt(0)\n\ts_barrier\n\ts_bitcmp1_b32 %[flg], 1\n\t" \
    "s_cbranch_scc0 10f\n\tv_mov_b32 v112, 1\n\ts_mov_b64 exec, 0xff\n\tbuffer_atomic_add v112, v117, %[rs], %[cso] offen\n\ts_mov_b64 exec, -1\n\t10:\n\ts_setprio 3\n\t" \
    "v_exp_f32 v32, v32\n\tv_exp_f32 v33, v33\n\tv_smfmac_f32_32x32x32_bf16 v[0:15], v[88:91], v[56:63], v92\n\tds_read_b128 v[68:71], v93 offset:2048\n\t" \
    "ds_read_b128 v[84:87], v94 offset:1024\n\tds_read_b128 v[88:91], v94 offset:1536\n\tv_exp_f32 v34, v34\n\tv_exp_f32 v35, v35\n\tv_exp_f32 v36, v36\n\ts_waitcnt lgkmcnt(3)\n\t" \
    "v_mfma_f32_32x32x16_bf16 v[48:63], v[72:75], v[64:67], v[16:31]\n\tv_exp_f32 v37, v37\n\tv_exp_f32 v38, v38\n\tv_exp_f32 v39, v39\n\tv_exp_f32 v40, v40\n\tv_exp_f32 v41, v41\n\t" \
    "v_exp_f32 v42, v42\n\tv_smfmac_f32_32x32x32_bf16 v[0:15], v[76:79], v[32:39], v92\n\tv_exp_f32 v43, v43\n\tv_exp_f32 v44, v44\n\tv_exp_f32 v45, v45\n\tv_exp_f32 v46, v46\n\t" \
    "v_exp_f32 v47, v47\n\tv_exp_f32 v48, v48\n\tv_exp_f32 v49, v49\n\tv_smfmac_f32_32x32x32_bf16 v[0:15], v[80:83], v[40:47], v92\n\tds_read_b128 v[72:75], v93 offset:3072\n\t" \
    "ds_read_b128 v[76:79], v94 offset:2048\n\tds_read_b128 v[80:83], v94 offset:2560\n\tv_exp_f32 v50, v50\n\tv_exp_f32 v51, v51\n\tv_exp_f32 v52, v52\n\ts_waitcnt lgkmcnt(3)\n\t" \
    "v_mfma_f32_32x32x16_bf16 v[32:47], v[68:71], v[64:67], v[16:31]\n\tv_exp_f32 v53, v53\n\tv_exp_f32 v54, v54\n\tv_exp_f32 v55, v55\n\tv_exp_f32 v56, v56\n\tv_exp_f32 v57, v57\n\t" \
    "v_exp_f32 v58, v58\n\tv_smfmac_f32_32x32x32_bf16 v[0:15], v[84:87], v[48:55], v92\n\tv_exp_f32 v59, v59\n\tv_exp_f32 v60, v60\n\tv_exp_f32 v61, v61\n\tv_exp_f32 v62, v62\n\t" \
    "v_exp_f32 v63, v63\n\tv_exp_f32 v32, v32\n\tv_exp_f32 v33, v33\n\tv_smfmac_f32_32x32x32_bf16 v[0:15], v[88:91], v[56:63], v92\n\tds_read_b128 v[68:71], v93 offset:4096\n\t" \
    "ds_read_b128 v[84:87], v94 offset:3072\n\tds_read_b128 v[88:91], v94 offset:3584\n\tv_exp_f32 v34, v34\n\tv_exp_f32 v35, v35\n\tv_exp_f32 v36, v36\n\ts_waitcnt lgkmcnt(3)\n\t" \
    "v_mfma_f32_32x32x16_bf16 v[48:63], v[72:75], v[64:67], v[16:31]\n\tv_exp_f32 v37, v37\n\tv_exp_f32 v38, v38\n\tv_exp_f32 v39, v39\n\tv_exp_f32 v40, v40\n\tv_exp_f32 v41, v41\n\t" \
    "v_exp_f32 v42, v42\n\tv_smfmac_f32_32x32x32_bf16 v[0:15], v[76:79], v[32:39], v92\n\tv_exp_f32 v43, v43\n\tv_exp_f32 v44, v44\n\tv_exp_f32 v45, v45\n\tv_exp_f32 v46, v46\n\t" \
    "v_exp_f32 v47, v47\n\tv_exp_f32 v48, v48\n\tv_exp_f32 v49, v49\n\tv_smfmac_f32_32x32x32_bf16 v[0:15], v[80:83], v[40:47], v92\n\tv_add_u32 v94, v94, v95\n\t" \
    "ds_read_b128 v[72:75], v93 offset:5120\n\tds_read_b128 v[76:79], v94 offset:0\n\tds_read_b128 v[80:83], v94 offset:512\n\tv_exp_f32 v50, v50\n\tv_exp_f32 v51, v51\n\t" \
    "v_exp_f32 v52, v52\n\ts_waitcnt lgkmcnt(3)\n\tv_mfma_f32_32x32x16_bf16 v[32:47], v[68:71], v[64:67], v[16:31]\n\tv_exp_f32 v53, v53\n\tv_exp_f32 v54, v54\n\tv_exp_f32 v55, v55\n\t" \
    "v_exp_f32 v56, v56\n\tv_exp_f32 v57, v57\n\tv_exp_f32 v58, v58\n\tv_smfmac_f32_32x32x32_bf16 v[0:15], v[84:87], v[48:55], v92\n\tv_exp_f32 v59, v59\n\tv_exp_f32 v60, v60\n\t" \
    "v_exp_f32 v61, v61\n\tv_exp_f32 v62, v62\n\tv_exp_f32 v63, v63\n\tv_add_u32 v93, 0x1000, v93\n\ts_setprio 2\n\tv_exp_f32 v32, v32\n\tv_exp_f32 v33, v33\n\t" \
    "v_smfmac_f32_32x32x32_bf16 v[0:15], v[88:91], v[56:63], v92\n\tds_read_b128 v[68:71], v93 offset:2048\n\tds_read_b128 v[84:87], v94 offset:1024\n\t" \
    "ds_read_b128 v[88:91], v94 offset:1536\n\tv_exp_f32 v34, v34\n\tv_exp_f32 v35, v35\n\tv_exp_f32 v36, v36\n\ts_waitcnt lgkmcnt(3)\n\t" \
    "v_mfma_f32_32x32x16_bf16 v[48:63], v[72:75], v[64:67], v[16:31]\n\tv_exp_f32 v37, v37\n\tv_exp_f32 v38, v38\n\tv_exp_f32 v39, v39\n\tv_exp_f32 v40, v40\n\tv_exp_f32 v41, v41\n\t" \
    "v_exp_f32 v42, v42\n\tv_smfmac_f32_32x32x32_bf16 v[0:15], v[76:79], v[32:39], v92\n\tv_exp_f32 v43, v43\n\tv_exp_f32 v44, v44\n\tv_exp_f32 v45, v45\n\tv_exp_f32 v46, v46\n\t" \
    "v_exp_f32 v47, v47\n\tv_exp_f32 v48, v48\n\tv_exp_f32 v49, v49\n\tv_smfmac_f32_32x32x32_bf16 v[0:15], v[80:83], v[40:47], v92\n\tds_read_b128 v[72:75], v93 offset:3072\n\t" \
    "ds_read_b128 v[76:79], v94 offset:2048\n\tds_read_b128 v[80:83], v94 offset:2560\n\tv_exp_f32 v50, v50\n\tv_exp_f32 v51, v51\n\tv_exp_f32 v52, v52\n\ts_waitcnt lgkmcnt(3)\n\t" \
    "v_mfma_f32_32x32x16_bf16 v[32:47], v[68:71], v[64:67], v[16:31]\n\tv_exp_f32 v53, v53\n\tv_exp_f32 v54, v54\n\tv_exp_f32 v55, v55\n\tv_exp_f32 v56, v56\n\tv_exp_f32 v57, v57\n\t" \
    "v_exp_f32 v58, v58\n\tv_smfmac_f32_32x32x32_bf16 v[0:15], v[84:87], v[48:55], v92\n\tv_exp_f32 v59, v59\n\tv_exp_f32 v60, v60\n\tv_exp_f32 v61, v61\n\tv_exp_f32 v62, v62\n\t" \
    "v_exp_f32 v63, v63\n\ts_setprio 1\n\tv_exp_f32 v32, v32\n\tv_exp_f32 v33, v33\n\tv_smfmac_f32_32x32x32_bf16 v[0:15], v[88:91], v[56:63], v92\n\t" \
    "ds_read_b128 v[68:71], v93 offset:4096\n\tds_read_b128 v[84:87], v94 offset:3072\n\tds_read_b128 v[88:91], v94 offset:3584\n\tv_exp_f32 v34, v34\n\tv_exp_f32 v35, v35\n\t" \
    "v_exp_f32 v36, v36\n\ts_waitcnt lgkmcnt(3)\n\tv_mfma_f32_32x32x16_bf16 v[48:63], v[72:75], v[64:67], v[16:31]\n\tv_exp_f32 v37, v37\n\tv_exp_f32 v38, v38\n\tv_exp_f32 v39, v39\n\t" \
    "v_exp_f32 v40, v40\n\tv_exp_f32 v41, v41\n\tv_exp_f32 v42, v42\n\tv_smfmac_f32_32x32x32_bf16 v[0:15], v[76:79], v[32:39], v92\n\tv_exp_f32 v43, v43\n\tv_exp_f32 v44, v44\n\t" \
    "v_exp_f32 v45, v45\n\tv_exp_f32 v46, v46\n\tv_exp_f32 v47, v47\n\tv_exp_f32 v48, v48\n\tv_exp_f32 v49, v49\n\tv_smfmac_f32_32x32x32_bf16 v[0:15], v[80:83], v[40:47], v92\n\t" \
    "v_add_u32 v94, v94, v95\n\tds_read_b128 v[72:75], v93 offset:5120\n\tds_read_b128 v[76:79], v94 offset:0\n\tds_read_b128 v[80:83], v94 offset:512\n\tv_exp_f32 v50, v50\n\t" \
    "v_exp_f32 v51, v51\n\tv_exp_f32 v52, v52\n\ts_waitcnt lgkmcnt(3)\n\tv_mfma_f32_32x32x16_bf16 v[32:47], v[68:71], v[64:67], v[16:31]\n\tv_exp_f32 v53, v53\n\tv_exp_f32 v54, v54\n\t" \
    "v_exp_f32 v55, v55\n\tv_exp_f32 v56, v56\n\tv_exp_f32 v57, v57\n\tv_exp_f32 v58, v58\n\tv_smfmac_f32_32x32x32_bf16 v[0:15], v[84:87], v[48:55], v92\n\tv_exp_f32 v59, v59\n\t" \
    "v_exp_f32 v60, v60\n\tv_exp_f32 v61, v61\n\tv_exp_f32 v62, v62\n\tv_exp_f32 v63, v63\n\tv_add_u32 v93, 0x1000, v93\n\ts_nop 1\n\t" \
    "v_smfmac_f32_32x32x32_bf16 v[0:15], v[88:91], v[56:63], v92\n\ts_waitcnt lgkmcnt(0)\n\ts_nop 7\n\ts_nop 7\n\ts_mov_b32 m0, s88\n\ts_setprio 0\n\t"
#define ATT_STAGE_B \
    "s_mov_b32 s88, m0\n\ts_mov_b32 s89, %[wbo]\n\ts_bitcmp1_b32 %[flg], 2\n\ts_cselect_b64 vcc, -1, 0\n\ts_setprio 3\n\tv_mov_b32 v93, %[ka]\n\tv_mov_b32 v94, %[va]\n\t" \
    "v_mov_b32 v95, %[vs]\n\tds_read_b128 v[68:71], v93\n\tds_read_b128 v[72:75], v93 offset:1024\n\tds_read_b128 v[76:79], v94\n\tds_read_b128 v[80:83], v94 offset:512\n\t" \
    "v_mov_b32 v16, %[negm]\n\tv_mov_b32 v17, %[negm]\n\tv_mov_b32 v18, %[negm]\n\tv_mov_b32 v19, %[negm]\n\tv_mov_b32 v20, %[negm]\n\tv_mov_b32 v21, %[negm]\n\t" \
    "v_mov_b32 v22, %[negm]\n\tv_mov_b32 v23, %[negm]\n\tv_mov_b32 v24, %[negm]\n\tv_mov_b32 v25, %[negm]\n\tv_mov_b32 v26, %[negm]\n\tv_mov_b32 v27, %[negm]\n\t" \
    "v_mov_b32 v28, %[negm]\n\tv_mov_b32 v29, %[negm]\n\tv_mov_b32 v30, %[negm]\n\tv_mov_b32 v31, %[negm]\n\tv_mov_b32 v92, 0xdddddddd\n\ts_waitcnt lgkmcnt(3)\n\t" \
    "v_mfma_f32_32x32x16_bf16 v[32:47], v[68:71], v[64:67], v[16:31]\n\tv_mov_b64 v[0:1], 0\n\tv_mov_b64 v[2:3], 0\n\tv_mov_b64 v[4:5], 0\n\tv_mov_b64 v[6:7], 0\n\t" \
    "v_mov_b64 v[8:9], 0\n\tv_mov_b64 v[10:11], 0\n\tv_mov_b64 v[12:13], 0\n\tv_mov_b64 v[14:15], 0\n\tv_mov_b64 v[56:57], 0\n\tv_mov_b64 v[58:59], 0\n\tv_mov_b64 v[60:61], 0\n\t" \
    "v_mov_b64 v[62:63], 0\n\tv_mov_b64 v[88:89], 0\n\tv_mov_b64 v[90:91], 0\n\ts_waitcnt lgkmcnt(0)\n\tv_exp_f32 v32, v32\n\tv_exp_f32 v33, v33\n\t" \
    "v_smfmac_f32_32x32x32_bf16 v[0:15], v[88:91], v[56:63], v92\n\ts_cbranch_vccz 11f\n\tbuffer_load_dwordx4 v[96:99], %[l16], %[rs], s89 offen offset:0 sc1\n\t" \
    "buffer_load_dwordx4 v[100:103], %[l16], %[rs], s89 offen offset:1024 sc1\n\t11:\n\tds_read_b128 v[68:71], v93 offset:2048\n\tds_read_b128 v[84:87], v94 offset:1024\n\t" \
    "ds_read_b128 v[88:91], v94 offset:1536\n\tv_exp_f32 v34, v34\n\tv_exp_f32 v35, v35\n\tv_exp_f32 v36, v36\n\ts_waitcnt lgkmcnt(3)\n\t" \
    "v_mfma_f32_32x32x16_bf16 v[48:63], v[72:75], v[64:67], v[16:31]\n\tv_exp_f32 v37, v37\n\tv_exp_f32 v38, v38\n\tv_exp_f32 v39, v39\n\tv_exp_f32 v40, v40\n\tv_exp_f32 v41, v41\n\t" \
    "v_exp_f32 v42, v42\n\tv_smfmac_f32_32x32x32_bf16 v[0:15], v[76:79], v[32:39], v92\n\tv_exp_f32 v43, v43\n\tv_exp_f32 v44, v44\n\tv_exp_f32 v45, v45\n\tv_exp_f32 v46, v46\n\t" \
    "v_exp_f32 v47, v47\n\tv_exp_f32 v48, v48\n\tv_exp_f32 v49, v49\n\tv_smfmac_f32_32x32x32_bf16 v[0:15], v[80:83], v[40:47], v92\n\ts_cbranch_vccz 12f\n\t" \
    "buffer_load_dwordx4 v[104:107], %[l16], %[rs], s89 offen offset:2048 sc1\n\tbuffer_load_dwordx4 v[108:111], %[l16], %[rs], s89 offen offset:3072 sc1\n\t" \
    "s_add_u32 s89, s89, 0x1000\n\t12:\n\tds_read_b128 v[72:75], v93 offset:3072\n\tds_read_b128 v[76:79], v94 offset:2048\n\tds_read_b128 v[80:83], v94 offset:2560\n\t" \
    "v_exp_f32 v50, v50\n\tv_exp_f32 v51, v51\n\tv_exp_f32 v52, v52\n\ts_waitcnt lgkmcnt(3)\n\tv_mfma_f32_32x32x16_bf16 v[32:47], v[68:71], v[64:67], v[16:31]\n\tv_exp_f32 v53, v53\n\t" \
    "v_exp_f32 v54, v54\n\tv_exp_f32 v55, v55\n\tv_exp_f32 v56, v56\n\tv_exp_f32 v57, v57\n\tv_exp_f32 v58, v58\n\tv_smfmac_f32_32x32x32_bf16 v[0:15], v[84:87], v[48:55], v92\n\t" \
    "v_exp_f32 v59, v59\n\tv_exp_f32 v60, v60\n\tv_exp_f32 v61, v61\n\tv_exp_f32 v62, v62\n\tv_exp_f32 v63, v63\n\tv_exp_f32 v32, v32\n\tv_exp_f32 v33, v33\n\t" \
    "v_smfmac_f32_32x32x32_bf16 v[0:15], v[88:91], v[56:63], v92\n\ts_cbranch_vccz 13f\n\tbuffer_load_dwordx4 v[112:115], %[l16], %[rs], s89 offen offset:0 sc1\n\t" \
    "buffer_load_dwordx4 v[116:119], %[l16], %[rs], s89 offen offset:1024 sc1\n\t13:\n\tds_read_b128 v[68:71], v93 offset:4096\n\tds_read_b128 v[84:87], v94 offset:3072\n\t" \
    "ds_read_b128 v[88:91], v94 offset:3584\n\tv_exp_f32 v34, v34\n\tv_exp_f32 v35, v35\n\tv_exp_f32 v36, v36\n\ts_waitcnt lgkmcnt(3)\n\t" \
    "v_mfma_f32_32x32x16_bf16 v[48:63], v[72:75], v[64:67], v[16:31]\n\tv_exp_f32 v37, v37\n\tv_exp_f32 v38, v38\n\tv_exp_f32 v39, v39\n\tv_exp_f32 v40, v40\n\tv_exp_f32 v41, v41\n\t" \
    "v_exp_f32 v42, v42\n\tv_smfmac_f32_32x32x32_bf16 v[0:15], v[76:79], v[32:39], v92\n\tv_exp_f32 v43, v43\n\tv_exp_f32 v44, v44\n\tv_exp_f32 v45, v45\n\tv_exp_f32 v46, v46\n\t" \
    "v_exp_f32 v47, v47\n\tv_exp_f32 v48, v48\n\tv_exp_f32 v49, v49\n\tv_smfmac_f32_32x32x32_bf16 v[0:15], v[80:83], v[40:47], v92\n\ts_cbranch_vccz 14f\n\t" \
    "buffer_load_dwordx4 v[120:123], %[l16], %[rs], s89 offen offset:2048 sc1\n\tbuffer_load_dwordx4 v[124:127], %[l16], %[rs], s89 offen offset:3072 sc1\n\t" \
    "s_add_u32 s89, s89, 0x1000\n\t14:\n\tv_add_u32 v94, v94, v95\n\tds_read_b128 v[72:75], v93 offset:5120\n\tds_read_b128 v[76:79], v94 offset:0\n\t" \
    "ds_read_b128 v[80:83], v94 offset:512\n\tv_exp_f32 v50, v50\n\tv_exp_f32 v51, v51\n\tv_exp_f32 v52, v52\n\ts_waitcnt lgkmcnt(3)\n\t" \
    "v_mfma_f32_32x32x16_bf16 v[32:47], v[68:71], v[64:67], v[16:31]\n\tv_exp_f32 v53, v53\n\tv_exp_f32 v54, v54\n\tv_exp_f32 v55, v55\n\tv_exp_f32 v56, v56\n\tv_exp_f32 v57, v57\n\t" \
    "v_exp_f32 v58, v58\n\tv_smfmac_f32_32x32x32_bf16 v[0:15], v[84:87], v[48:55], v92\n\tv_exp_f32 v59, v59\n\tv_exp_f32 v60, v60\n\tv_exp_f32 v61, v61\n\tv_exp_f32 v62, v62\n\t" \
    "v_exp_f32 v63, v63\n\tv_add_u32 v93, 0x1000, v93\n\tv_exp_f32 v32, v32\n\tv_exp_f32 v33, v33\n\tv_smfmac_f32_32x32x32_bf16 v[0:15], v[88:91], v[56:63], v92\n\t" \
    "s_cbranch_vccz 15f\n\tbuffer_load_dwordx4 v[128:131], %[l16], %[rs], s89 offen offset:0 sc1\n\tbuffer_load_dwordx4 v[132:135], %[l16], %[rs], s89 offen offset:1024 sc1\n\t15:\n\t" \
    "ds_read_b128 v[68:71], v93 offset:2048\n\tds_read_b128 v[84:87], v94 offset:1024\n\tds_read_b128 v[88:91], v94 offset:1536\n\tv_exp_f32 v34, v34\n\tv_exp_f32 v35, v35\n\t" \
    "v_exp_f32 v36, v36\n\ts_waitcnt lgkmcnt(3)\n\tv_mfma_f32_32x32x16_bf16 v[48:63], v[72:75], v[64:67], v[16:31]\n\tv_exp_f32 v37, v37\n\tv_exp_f32 v38, v38\n\tv_exp_f32 v39, v39\n\t" \
    "v_exp_f32 v40, v40\n\tv_exp_f32 v41, v41\n\tv_exp_f32 v42, v42\n\tv_smfmac_f32_32x32x32_bf16 v[0:15], v[76:79], v[32:39], v92\n\tv_exp_f32 v43, v43\n\tv_exp_f32 v44, v44\n\t" \
    "v_exp_f32 v45, v45\n\tv_exp_f32 v46, v46\n\tv_exp_f32 v47, v47\n\tv_exp_f32 v48, v48\n\tv_exp_f32 v49, v49\n\tv_smfmac_f32_32x32x32_bf16 v[0:15], v[80:83], v[40:47], v92\n\t" \
    "s_cbranch_vccz 16f\n\tbuffer_load_dwordx4 v[136:139], %[l16], %[rs], s89 offen offset:2048 sc1\n\tbuffer_load_dwordx4 v[140:143], %[l16], %[rs], s89 offen offset:3072 sc1\n\t" \
    "s_add_u32 s89, s89, 0x1000\n\t16:\n\tds_read_b128 v[72:75], v93 offset:3072\n\tds_read_b128 v[76:79], v94 offset:2048\n\tds_read_b128 v[80:83], v94 offset:2560\n\t" \
    "v_exp_f32 v50, v50\n\tv_exp_f32 v51, v51\n\tv_exp_f32 v52, v52\n\ts_waitcnt lgkmcnt(3)\n\tv_mfma_f32_32x32x16_bf16 v[32:47], v[68:71], v[64:67], v[16:31]\n\tv_exp_f32 v53, v53\n\t" \
    "v_exp_f32 v54, v54\n\tv_exp_f32 v55, v55\n\tv_exp_f32 v56, v56\n\tv_exp_f32 v57, v57\n\tv_exp_f32 v58, v58\n\tv_smfmac_f32_32x32x32_bf16 v[0:15], v[84:87], v[48:55], v92\n\t" \
    "v_exp_f32 v59, v59\n\tv_exp_f32 v60, v60\n\tv_exp_f32 v61, v61\n\tv_exp_f32 v62, v62\n\tv_exp_f32 v63, v63\n\tv_exp_f32 v32, v32\n\tv_exp_f32 v33, v33\n\t" \
    "v_smfmac_f32_32x32x32_bf16 v[0:15], v[88:91], v[56:63], v92\n\ts_cbranch_vccz 17f\n\tbuffer_load_dwordx4 v[144:147], %[l16], %[rs], s89 offen offset:0 sc1\n\t" \
    "buffer_load_dwordx4 v[148:151], %[l16], %[rs], s89 offen offset:1024 sc1\n\t17:\n\tds_read_b128 v[68:71], v93 offset:4096\n\tds_read_b128 v[84:87], v94 offset:3072\n\t" \
    "ds_read_b128 v[88:91], v94 offset:3584\n\tv_exp_f32 v34, v34\n\tv_exp_f32 v35, v35\n\tv_exp_f32 v36, v36\n\ts_waitcnt lgkmcnt(3)\n\t" \
    "v_mfma_f32_32x32x16_bf16 v[48:63], v[72:75], v[64:67], v[16:31]\n\tv_exp_f32 v37, v37\n\tv_exp_f32 v38, v38\n\tv_exp_f32 v39, v39\n\tv_exp_f32 v40, v40\n\tv_exp_f32 v41, v41\n\t" \
    "v_exp_f32 v42, v42\n\tv_smfmac_f32_32x32x32_bf16 v[0:15], v[76:79], v[32:39], v92\n\tv_exp_f32 v43, v43\n\tv_exp_f32 v44, v44\n\tv_exp_f32 v45, v45\n\tv_exp_f32 v46, v46\n\t" \
    "v_exp_f32 v47, v47\n\tv_exp_f32 v48, v48\n\tv_exp_f32 v49, v49\n\tv_smfmac_f32_32x32x32_bf16 v[0:15], v[80:83], v[40:47], v92\n\ts_cbranch_vccz 18f\n\t" \
    "buffer_load_dwordx4 v[152:155], %[l16], %[rs], s89 offen offset:2048 sc1\n\tbuffer_load_dwordx4 v[156:159], %[l16], %[rs], s89 offen offset:3072 sc1\n\t" \
    "s_add_u32 s89, s89, 0x1000\n\t18:\n\tv_add_u32 v94, v94, v95\n\tds_read_b128 v[72:75], v93 offset:5120\n\tds_read_b128 v[76:79], v94 offset:0\n\t" \
    "ds_read_b128 v[80:83], v94 offset:512\n\tv_exp_f32 v50, v50\n\tv_exp_f32 v51, v51\n\tv_exp_f32 v52, v52\n\ts_waitcnt lgkmcnt(3)\n\t" \
    "v_mfma_f32_32x32x16_bf16 v[32:47], v[68:71], v[64:67], v[16:31]\n\tv_exp_f32 v53, v53\n\tv_exp_f32 v54, v54\n\tv_exp_f32 v55, v55\n\tv_exp_f32 v56, v56\n\tv_exp_f32 v57, v57\n\t" \
    "v_exp_f32 v58, v58\n\tv_smfmac_f32_32x32x32_bf16 v[0:15], v[84:87], v[48:55], v92\n\tv_exp_f32 v59, v59\n\tv_exp_f32 v60, v60\n\tv_exp_f32 v61, v61\n\tv_exp_f32 v62, v62\n\t" \
    "v_exp_f32 v63, v63\n\tv_add_u32 v93, 0x1000, v93\n\tv_exp_f32 v32, v32\n\tv_exp_f32 v33, v33\n\tv_smfmac_f32_32x32x32_bf16 v[0:15], v[88:91], v[56:63], v92\n\t" \
    "s_cbranch_vccz 19f\n\tbuffer_load_dwordx4 v[160:163], %[l16], %[rs], s89 offen offset:0 sc1\n\tbuffer_load_dwordx4 v[164:167], %[l16], %[rs], s89 offen offset:1024 sc1\n\t19:\n\t" \
    "ds_read_b128 v[68:71], v93 offset:2048\n\tds_read_b128 v[84:87], v94 offset:1024\n\tds_read_b128 v[88:91], v94 offset:1536\n\tv_exp_f32 v34, v34\n\tv_exp_f32 v35, v35\n\t" \
    "v_exp_f32 v36, v36\n\ts_waitcnt lgkmcnt(3)\n\tv_mfma_f32_32x32x16_bf16 v[48:63], v[72:75], v[64:67], v[16:31]\n\tv_exp_f32 v37, v37\n\tv_exp_f32 v38, v38\n\tv_exp_f32 v39, v39\n\t" \
    "v_exp_f32 v40, v40\n\tv_exp_f32 v41, v41\n\tv_exp_f32 v42, v42\n\tv_smfmac_f32_32x32x32_bf16 v[0:15], v[76:79], v[32:39], v92\n\tv_exp_f32 v43, v43\n\tv_exp_f32 v44, v44\n\t" \
    "v_exp_f32 v45, v45\n\tv_exp_f32 v46, v46\n\tv_exp_f32 v47, v47\n\tv_exp_f32 v48, v48\n\tv_exp_f32 v49, v49\n\tv_smfmac_f32_32x32x32_bf16 v[0:15], v[80:83], v[40:47], v92\n\t" \
    "s_cbranch_vccz 20f\n\tbuffer_load_dwordx4 v[168:171], %[l16], %[rs], s89 offen offset:2048 sc1\n\tbuffer_load_dwordx4 v[172:175], %[l16], %[rs], s89 offen offset:3072 sc1\n\t" \
    "s_add_u32 s89, s89, 0x1000\n\t20:\n\tds_read_b128 v[72:75], v93 offset:3072\n\tds_read_b128 v[76:79], v94 offset:2048\n\tds_read_b128 v[80:83], v94 offset:2560\n\t" \
    "v_exp_f32 v50, v50\n\tv_exp_f32 v51, v51\n\tv_exp_f32 v52, v52\n\ts_waitcnt lgkmcnt(3)\n\tv_mfma_f32_32x32x16_bf16 v[32:47], v[68:71], v[64:67], v[16:31]\n\tv_exp_f32 v53, v53\n\t" \
    "v_exp_f32 v54, v54\n\tv_exp_f32 v55, v55\n\tv_exp_f32 v56, v56\n\tv_exp_f32 v57, v57\n\tv_exp_f32 v58, v58\n\tv_smfmac_f32_32x32x32_bf16 v[0:15], v[84:87], v[48:55], v92\n\t" \
    "v_exp_f32 v59, v59\n\tv_exp_f32 v60, v60\n\tv_exp_f32 v61, v61\n\tv_exp_f32 v62, v62\n\tv_exp_f32 v63, v63\n\tv_exp_f32 v32, v32\n\tv_exp_f32 v33, v33\n\t" \
    "v_smfmac_f32_32x32x32_bf16 v[0:15], v[88:91], v[56:63], v92\n\ts_cbranch_vccz 21f\n\tbuffer_load_dwordx4 v[176:179], %[l16], %[rs], s89 offen offset:0 sc1\n\t" \
    "buffer_load_dwordx4 v[180:183], %[l16], %[rs], s89 offen offset:1024 sc1\n\t21:\n\tds_read_b128 v[68:71], v93 offset:4096\n\tds_read_b128 v[84:87], v94 offset:3072\n\t" \
    "ds_read_b128 v[88:91], v94 offset:3584\n\tv_exp_f32 v34, v34\n\tv_exp_f32 v35, v35\n\tv_exp_f32 v36, v36\n\ts_waitcnt lgkmcnt(3)\n\t" \
    "v_mfma_f32_32x32x16_bf16 v[48:63], v[72:75], v[64:67], v[16:31]\n\tv_exp_f32 v37, v37\n\tv_exp_f32 v38, v38\n\tv_exp_f32 v39, v39\n\tv_exp_f32 v40, v40\n\tv_exp_f32 v41, v41\n\t" \
    "v_exp_f32 v42, v42\n\tv_smfmac_f32_32x32x32_bf16 v[0:15], v[76:79], v[32:39], v92\n\tv_exp_f32 v43, v43\n\tv_exp_f32 v44, v44\n\tv_exp_f32 v45, v45\n\tv_exp_f32 v46, v46\n\t" \
    "v_exp_f32 v47, v47\n\tv_exp_f32 v48, v48\n\tv_exp_f32 v49, v49\n\tv_smfmac_f32_32x32x32_bf16 v[0:15], v[80:83], v[40:47], v92\n\ts_cbranch_vccz 22f\n\t" \
    "buffer_load_dwordx4 v[184:187], %[l16], %[rs], s89 offen offset:2048 sc1\n\tbuffer_load_dwordx4 v[188:191], %[l16], %[rs], s89 offen offset:3072 sc1\n\t" \
    "s_add_u32 s89, s89, 0x1000\n\t22:\n\tv_add_u32 v94, v94, v95\n\tds_read_b128 v[72:75], v93 offset:5120\n\tds_read_b128 v[76:79], v94 offset:0\n\t" \
    "ds_read_b128 v[80:83], v94 offset:512\n\tv_exp_f32 v50, v50\n\tv_exp_f32 v51, v51\n\tv_exp_f32 v52, v52\n\ts_waitcnt lgkmcnt(3)\n\t" \
    "v_mfma_f32_32x32x16_bf16 v[32:47], v[68:71], v[64:67], v[16:31]\n\tv_exp_f32 v53, v53\n\tv_exp_f32 v54, v54\n\tv_exp_f32 v55, v55\n\tv_exp_f32 v56, v56\n\tv_exp_f32 v57, v57\n\t" \
    "v_exp_f32 v58, v58\n\tv_smfmac_f32_32x32x32_bf16 v[0:15], v[84:87], v[48:55], v92\n\tv_exp_f32 v59, v59\n\tv_exp_f32 v60, v60\n\tv_exp_f32 v61, v61\n\tv_exp_f32 v62, v62\n\t" \
    "v_exp_f32 v63, v63\n\tv_add_u32 v93, 0x1000, v93\n\ts_mov_b64 s[86:87], %[wds]\n\ts_mov_b32 m0, %[snk]\n\tv_exp_f32 v32, v32\n\tv_exp_f32 v33, v33\n\t" \
    "v_smfmac_f32_32x32x32_bf16 v[0:15], v[88:91], v[56:63], v92\n\ts_cbranch_vccz 23f\n\tbuffer_load_dwordx4 v[192:195], %[l16], %[rs], s89 offen offset:0 sc1\n\t23:\n\t" \
    "global_load_lds_dwordx4 %[l16], s[86:87]\n\ts_add_u32 s86, s86, 0x400\n\ts_addc_u32 s87, s87, 0\n\tds_read_b128 v[68:71], v93 offset:2048\n\t" \
    "ds_read_b128 v[84:87], v94 offset:1024\n\tds_read_b128 v[88:91], v94 offset:1536\n\tv_exp_f32 v34, v34\n\tv_exp_f32 v35, v35\n\tv_exp_f32 v36, v36\n\ts_waitcnt lgkmcnt(3)\n\t" \
    "v_mfma_f32_32x32x16_bf16 v[48:63], v[72:75], v[64:67], v[16:31]\n\tv_exp_f32 v37, v37\n\tv_exp_f32 v38, v38\n\tv_exp_f32 v39, v39\n\tv_exp_f32 v40, v40\n\tv_exp_f32 v41, v41\n\t" \
    "v_exp_f32 v42, v42\n\tv_smfmac_f32_32x32x32_bf16 v[0:15], v[76:79], v[32:39], v92\n\tv_exp_f32 v43, v43\n\tv_exp_f32 v44, v44\n\tv_exp_f32 v45, v45\n\tv_exp_f32 v46, v46\n\t" \
    "v_exp_f32 v47, v47\n\tv_exp_f32 v48, v48\n\tv_exp_f32 v49, v49\n\tv_smfmac_f32_32x32x32_bf16 v[0:15], v[80:83], v[40:47], v92\n\ts_cbranch_vccz 24f\n\t" \
    "buffer_load_dwordx4 v[196:199], %[l16], %[rs], s89 offen offset:1024 sc1\n\t24:\n\tglobal_load_lds_dwordx4 %[l16], s[86:87]\n\ts_add_u32 s86, s86, 0x400\n\t" \
    "s_addc_u32 s87, s87, 0\n\tds_read_b128 v[72:75], v93 offset:3072\n\tds_read_b128 v[76:79], v94 offset:2048\n\tds_read_b128 v[80:83], v94 offset:2560\n\tv_exp_f32 v50, v50\n\t" \
    "v_exp_f32 v51, v51\n\tv_exp_f32 v52, v52\n\ts_waitcnt lgkmcnt(3)\n\tv_mfma_f32_32x32x16_bf16 v[32:47], v[68:71], v[64:67], v[16:31]\n\tv_exp_f32 v53, v53\n\tv_exp_f32 v54, v54\n\t" \
    "v_exp_f32 v55, v55\n\tv_exp_f32 v56, v56\n\tv_exp_f32 v57, v57\n\tv_exp_f32 v58, v58\n\tv_smfmac_f32_32x32x32_bf16 v[0:15], v[84:87], v[48:55], v92\n\tv_exp_f32 v59, v59\n\t" \
    "v_exp_f32 v60, v60\n\tv_exp_f32 v61, v61\n\tv_exp_f32 v62, v62\n\tv_exp_f32 v63, v63\n\ts_setprio 2\n\tv_exp_f32 v32, v32\n\tv_exp_f32 v33, v33\n\t" \
    "v_smfmac_f32_32x32x32_bf16 v[0:15], v[88:91], v[56:63], v92\n\ts_cbranch_vccz 25f\n\tbuffer_load_dwordx4 v[200:203], %[l16], %[rs], s89 offen offset:2048 sc1\n\t25:\n\t" \
    "global_load_lds_dwordx4 %[l16], s[86:87]\n\ts_add_u32 s86, s86, 0x400\n\ts_addc_u32 s87, s87, 0\n\tds_read_b128 v[68:71], v93 offset:4096\n\t" \
    "ds_read_b128 v[84:87], v94 offset:3072\n\tds_read_b128 v[88:91], v94 offset:3584\n\tv_exp_f32 v34, v34\n\tv_exp_f32 v35, v35\n\tv_exp_f32 v36, v36\n\ts_waitcnt lgkmcnt(3)\n\t" \
    "v_mfma_f32_32x32x16_bf16 v[48:63], v[72:75], v[64:67], v[16:31]\n\tv_exp_f32 v37, v37\n\tv_exp_f32 v38, v38\n\tv_exp_f32 v39, v39\n\tv_exp_f32 v40, v40\n\tv_exp_f32 v41, v41\n\t" \
    "v_exp_f32 v42, v42\n\tv_smfmac_f32_32x32x32_bf16 v[0:15], v[76:79], v[32:39], v92\n\tv_exp_f32 v43, v43\n\tv_exp_f32 v44, v44\n\tv_exp_f32 v45, v45\n\tv_exp_f32 v46, v46\n\t" \
    "v_exp_f32 v47, v47\n\tv_exp_f32 v48, v48\n\tv_exp_f32 v49, v49\n\tv_smfmac_f32_32x32x32_bf16 v[0:15], v[80:83], v[40:47], v92\n\ts_cbranch_vccz 26f\n\t" \
    "buffer_load_dwordx4 v[204:207], %[l16], %[rs], s89 offen offset:3072 sc1\n\ts_add_u32 s89, s89, 0x1000\n\t26:\n\tglobal_load_lds_dwordx4 %[l16], s[86:87]\n\t" \
    "s_add_u32 s86, s86, 0x400\n\ts_addc_u32 s87, s87, 0\n\tv_add_u32 v94, v94, v95\n\tds_read_b128 v[72:75], v93 offset:5120\n\tds_read_b128 v[76:79], v94 offset:0\n\t" \
    "ds_read_b128 v[80:83], v94 offset:512\n\tv_exp_f32 v50, v50\n\tv_exp_f32 v51, v51\n\tv_exp_f32 v52, v52\n\ts_waitcnt lgkmcnt(3)\n\t" \
    "v_mfma_f32_32x32x16_bf16 v[32:47], v[68:71], v[64:67], v[16:31]\n\tv_exp_f32 v53, v53\n\tv_exp_f32 v54, v54\n\tv_exp_f32 v55, v55\n\tv_exp_f32 v56, v56\n\tv_exp_f32 v57, v57\n\t" \
    "v_exp_f32 v58, v58\n\tv_smfmac_f32_32x32x32_bf16 v[0:15], v[84:87], v[48:55], v92\n\tv_exp_f32 v59, v59\n\tv_exp_f32 v60, v60\n\tv_exp_f32 v61, v61\n\tv_exp_f32 v62, v62\n\t" \
    "v_exp_f32 v63, v63\n\tv_add_u32 v93, 0x1000, v93\n\tv_exp_f32 v32, v32\n\tv_exp_f32 v33, v33\n\tv_smfmac_f32_32x32x32_bf16 v[0:15], v[88:91], v[56:63], v92\n\t" \
    "s_cbranch_vccz 27f\n\tbuffer_load_dwordx4 v[208:211], %[l16], %[rs], s89 offen offset:0 sc1\n\t27:\n\tglobal_load_lds_dwordx4 %[l16], s[86:87]\n\ts_add_u32 s86, s86, 0x400\n\t" \
    "s_addc_u32 s87, s87, 0\n\tds_read_b128 v[68:71], v93 offset:2048\n\tds_read_b128 v[84:87], v94 offset:1024\n\tds_read_b128 v[88:91], v94 offset:1536\n\tv_exp_f32 v34, v34\n\t" \
    "v_exp_f32 v35, v35\n\tv_exp_f32 v36, v36\n\ts_waitcnt lgkmcnt(3)\n\tv_mfma_f32_32x32x16_bf16 v[48:63], v[72:75], v[64:67], v[16:31]\n\tv_exp_f32 v37, v37\n\tv_exp_f32 v38, v38\n\t" \
    "v_exp_f32 v39, v39\n\tv_exp_f32 v40, v40\n\tv_exp_f32 v41, v41\n\tv_exp_f32 v42, v42\n\tv_smfmac_f32_32x32x32_bf16 v[0:15], v[76:79], v[32:39], v92\n\tv_exp_f32 v43, v43\n\t" \
    "v_exp_f32 v44, v44\n\tv_exp_f32 v45, v45\n\tv_exp_f32 v46, v46\n\tv_exp_f32 v47, v47\n\tv_exp_f32 v48, v48\n\tv_exp_f32 v49, v49\n\t" \
    "v_smfmac_f32_32x32x32_bf16 v[0:15], v[80:83], v[40:47], v92\n\ts_cbranch_vccz 28f\n\tbuffer_load_dwordx4 v[212:215], %[l16], %[rs], s89 offen offset:1024 sc1\n\t28:\n\t" \
    "global_load_lds_dwordx4 %[l16], s[86:87]\n\ts_add_u32 s86, s86, 0x400\n\ts_addc_u32 s87, s87, 0\n\tds_read_b128 v[72:75], v93 offset:3072\n\t" \
    "ds_read_b128 v[76:79], v94 offset:2048\n\tds_read_b128 v[80:83], v94 offset:2560\n\tv_exp_f32 v50, v50\n\tv_exp_f32 v51, v51\n\tv_exp_f32 v52, v52\n\ts_waitcnt lgkmcnt(3)\n\t" \
    "v_mfma_f32_32x32x16_bf16 v[32:47], v[68:71], v[64:67], v[16:31]\n\tv_exp_f32 v53, v53\n\tv_exp_f32 v54, v54\n\tv_exp_f32 v55, v55\n\tv_exp_f32 v56, v56\n\tv_exp_f32 v57, v57\n\t" \
    "v_exp_f32 v58, v58\n\tv_smfmac_f32_32x32x32_bf16 v[0:15], v[84:87], v[48:55], v92\n\tv_exp_f32 v59, v59\n\tv_exp_f32 v60, v60\n\tv_exp_f32 v61, v61\n\tv_exp_f32 v62, v62\n\t" \
    "v_exp_f32 v63, v63\n\ts_setprio 1\n\tv_exp_f32 v32, v32\n\tv_exp_f32 v33, v33\n\tv_smfmac_f32_32x32x32_bf16 v[0:15], v[88:91], v[56:63], v92\n\ts_cbranch_vccz 29f\n\t" \
    "buffer_load_dwordx4 v[216:219], %[l16], %[rs], s89 offen offset:2048 sc1\n\t29:\n\tglobal_load_lds_dwordx4 %[l16], s[86:87]\n\ts_add_u32 s86, s86, 0x400\n\t" \
    "s_addc_u32 s87, s87, 0\n\tds_read_b128 v[68:71], v93 offset:4096\n\tds_read_b128 v[84:87], v94 offset:3072\n\tds_read_b128 v[88:91], v94 offset:3584\n\tv_exp_f32 v34, v34\n\t" \
    "v_exp_f32 v35, v35\n\tv_exp_f32 v36, v36\n\ts_waitcnt lgkmcnt(3)\n\tv_mfma_f32_32x32x16_bf16 v[48:63], v[72:75], v[64:67], v[16:31]\n\tv_exp_f32 v37, v37\n\tv_exp_f32 v38, v38\n\t" \
    "v_exp_f32 v39, v39\n\tv_exp_f32 v40, v40\n\tv_exp_f32 v41, v41\n\tv_exp_f32 v42, v42\n\tv_smfmac_f32_32x32x32_bf16 v[0:15], v[76:79], v[32:39], v92\n\tv_exp_f32 v43, v43\n\t" \
    "v_exp_f32 v44, v44\n\tv_exp_f32 v45, v45\n\tv_exp_f32 v46, v46\n\tv_exp_f32 v47, v47\n\tv_exp_f32 v48, v48\n\tv_exp_f32 v49, v49\n\t" \
    "v_smfmac_f32_32x32x32_bf16 v[0:15], v[80:83], v[40:47], v92\n\ts_cbranch_vccz 30f\n\tbuffer_load_dwordx4 v[220:223], %[l16], %[rs], s89 offen offset:3072 sc1\n\t" \
    "s_add_u32 s89, s89, 0x1000\n\t30:\n\ts_mov_b32 m0, %[xd]\n\ts_nop 0\n\tglobal_load_lds_dwordx4 %[xv], %[xs]\n\tv_add_u32 v94, v94, v95\n\tds_read_b128 v[72:75], v93 offset:5120\n\t" \
    "ds_read_b128 v[76:79], v94 offset:0\n\tds_read_b128 v[80:83], v94 offset:512\n\tv_exp_f32 v50, v50\n\tv_exp_f32 v51, v51\n\tv_exp_f32 v52, v52\n\ts_waitcnt lgkmcnt(3)\n\t" \
    "v_mfma_f32_32x32x16_bf16 v[32:47], v[68:71], v[64:67], v[16:31]\n\tv_exp_f32 v53, v53\n\tv_exp_f32 v54, v54\n\tv_exp_f32 v55, v55\n\tv_exp_f32 v56, v56\n\tv_exp_f32 v57, v57\n\t" \
    "v_exp_f32 v58, v58\n\tv_smfmac_f32_32x32x32_bf16 v[0:15], v[84:87], v[48:55], v92\n\tv_exp_f32 v59, v59\n\tv_exp_f32 v60, v60\n\tv_exp_f32 v61, v61\n\tv_exp_f32 v62, v62\n\t" \
    "v_exp_f32 v63, v63\n\tv_add_u32 v93, 0x1000, v93\n\ts_barrier\n\ts_setprio 3\n\ts_mov_b64 s[86:87], %[wos]\n\ts_mov_b32 m0, %[wod]\n\tv_exp_f32 v32, v32\n\tv_exp_f32 v33, v33\n\t" \
    "v_smfmac_f32_32x32x32_bf16 v[0:15], v[88:91], v[56:63], v92\n\tglobal_load_lds_dwordx4 %[l16], s[86:87] offset:0 sc1\n\tds_read_b128 v[68:71], v93 offset:2048\n\t" \
    "ds_read_b128 v[84:87], v94 offset:1024\n\tds_read_b128 v[88:91], v94 offset:1536\n\tv_exp_f32 v34, v34\n\tv_exp_f32 v35, v35\n\tv_exp_f32 v36, v36\n\ts_waitcnt lgkmcnt(3)\n\t" \
    "v_mfma_f32_32x32x16_bf16 v[48:63], v[72:75], v[64:67], v[16:31]\n\tv_exp_f32 v37, v37\n\tv_exp_f32 v38, v38\n\tv_exp_f32 v39, v39\n\tv_exp_f32 v40, v40\n\tv_exp_f32 v41, v41\n\t" \
    "v_exp_f32 v42, v42\n\tv_smfmac_f32_32x32x32_bf16 v[0:15], v[76:79], v[32:39], v92\n\tv_exp_f32 v43, v43\n\tv_exp_f32 v44, v44\n\tv_exp_f32 v45, v45\n\tv_exp_f32 v46, v46\n\t" \
    "v_exp_f32 v47, v47\n\tv_exp_f32 v48, v48\n\tv_exp_f32 v49, v49\n\tv_smfmac_f32_32x32x32_bf16 v[0:15], v[80:83], v[40:47], v92\n\t" \
    "global_load_lds_dwordx4 %[l16], s[86:87] offset:1024 sc1\n\tds_read_b128 v[72:75], v93 offset:3072\n\tds_read_b128 v[76:79], v94 offset:2048\n\t" \
    "ds_read_b128 v[80:83], v94 offset:2560\n\tv_exp_f32 v50, v50\n\tv_exp_f32 v51, v51\n\tv_exp_f32 v52, v52\n\ts_waitcnt lgkmcnt(3)\n\t" \
    "v_mfma_f32_32x32x16_bf16 v[32:47], v[68:71], v[64:67], v[16:31]\n\tv_exp_f32 v53, v53\n\tv_exp_f32 v54, v54\n\tv_exp_f32 v55, v55\n\tv_exp_f32 v56, v56\n\tv_exp_f32 v57, v57\n\t" \
    "v_exp_f32 v58, v58\n\tv_smfmac_f32_32x32x32_bf16 v[0:15], v[84:87], v[48:55], v92\n\tv_exp_f32 v59, v59\n\tv_exp_f32 v60, v60\n\tv_exp_f32 v61, v61\n\tv_exp_f32 v62, v62\n\t" \
    "v_exp_f32 v63, v63\n\tv_exp_f32 v32, v32\n\tv_exp_f32 v33, v33\n\tv_smfmac_f32_32x32x32_bf16 v[0:15], v[88:91], v[56:63], v92\n\t" \
    "global_load_lds_dwordx4 %[l16], s[86:87] offset:2048 sc1\n\tds_read_b128 v[68:71], v93 offset:4096\n\tds_read_b128 v[84:87], v94 offset:3072\n\t" \
    "ds_read_b128 v[88:91], v94 offset:3584\n\tv_exp_f32 v34, v34\n\tv_exp_f32 v35, v35\n\tv_exp_f32 v36, v36\n\ts_waitcnt lgkmcnt(3)\n\t" \
    "v_mfma_f32_32x32x16_bf16 v[48:63], v[72:75], v[64:67], v[16:31]\n\tv_exp_f32 v37, v37\n\tv_exp_f32 v38, v38\n\tv_exp_f32 v39, v39\n\tv_exp_f32 v40, v40\n\tv_exp_f32 v41, v41\n\t" \
    "v_exp_f32 v42, v42\n\tv_smfmac_f32_32x32x32_bf16 v[0:15], v[76:79], v[32:39], v92\n\tv_exp_f32 v43, v43\n\tv_exp_f32 v44, v44\n\tv_exp_f32 v45, v45\n\tv_exp_f32 v46, v46\n\t" \
    "v_exp_f32 v47, v47\n\tv_exp_f32 v48, v48\n\tv_exp_f32 v49, v49\n\tv_smfmac_f32_32x32x32_bf16 v[0:15], v[80:83], v[40:47], v92\n\t" \
    "global_load_lds_dwordx4 %[l16], s[86:87] offset:3072 sc1\n\ts_add_u32 s86, s86, 0x8000\n\ts_addc_u32 s87, s87, 0\n\ts_add_u32 s89, %[wod], 0x10000\n\ts_mov_b32 m0, s89\n\t" \
    "v_add_u32 v94, v94, v95\n\tds_read_b128 v[72:75], v93 offset:5120\n\tds_read_b128 v[76:79], v94 offset:0\n\tds_read_b128 v[80:83], v94 offset:512\n\tv_exp_f32 v50, v50\n\t" \
    "v_exp_f32 v51, v51\n\tv_exp_f32 v52, v52\n\ts_waitcnt lgkmcnt(3)\n\tv_mfma_f32_32x32x16_bf16 v[32:47], v[68:71], v[64:67], v[16:31]\n\tv_exp_f32 v53, v53\n\tv_exp_f32 v54, v54\n\t" \
    "v_exp_f32 v55, v55\n\tv_exp_f32 v56, v56\n\tv_exp_f32 v57, v57\n\tv_exp_f32 v58, v58\n\tv_smfmac_f32_32x32x32_bf16 v[0:15], v[84:87], v[48:55], v92\n\tv_exp_f32 v59, v59\n\t" \
    "v_exp_f32 v60, v60\n\tv_exp_f32 v61, v61\n\tv_exp_f32 v62, v62\n\tv_exp_f32 v63, v63\n\tv_add_u32 v93, 0x1000, v93\n\tv_exp_f32 v32, v32\n\tv_exp_f32 v33, v33\n\t" \
    "v_smfmac_f32_32x32x32_bf16 v[0:15], v[88:91], v[56:63], v92\n\tglobal_load_lds_dwordx4 %[l16], s[86:87] offset:0 sc1\n\tds_read_b128 v[68:71], v93 offset:2048\n\t" \
    "ds_read_b128 v[84:87], v94 offset:1024\n\tds_read_b128 v[88:91], v94 offset:1536\n\tv_exp_f32 v34, v34\n\tv_exp_f32 v35, v35\n\tv_exp_f32 v36, v36\n\ts_waitcnt lgkmcnt(3)\n\t" \
    "v_mfma_f32_32x32x16_bf16 v[48:63], v[72:75], v[64:67], v[16:31]\n\tv_exp_f32 v37, v37\n\tv_exp_f32 v38, v38\n\tv_exp_f32 v39, v39\n\tv_exp_f32 v40, v40\n\tv_exp_f32 v41, v41\n\t" \
    "v_exp_f32 v42, v42\n\tv_smfmac_f32_32x32x32_bf16 v[0:15], v[76:79], v[32:39], v92\n\tv_exp_f32 v43, v43\n\tv_exp_f32 v44, v44\n\tv_exp_f32 v45, v45\n\tv_exp_f32 v46, v46\n\t" \
    "v_exp_f32 v47, v47\n\tv_exp_f32 v48, v48\n\tv_exp_f32 v49, v49\n\tv_smfmac_f32_32x32x32_bf16 v[0:15], v[80:83], v[40:47], v92\n\t" \
    "global_load_lds_dwordx4 %[l16], s[86:87] offset:1024 sc1\n\tds_read_b128 v[72:75], v93 offset:3072\n\tds_read_b128 v[76:79], v94 offset:2048\n\t" \
    "ds_read_b128 v[80:83], v94 offset:2560\n\tv_exp_f32 v50, v50\n\tv_exp_f32 v51, v51\n\tv_exp_f32 v52, v52\n\ts_waitcnt lgkmcnt(3)\n\t" \
    "v_mfma_f32_32x32x16_bf16 v[32:47], v[68:71], v[64:67], v[16:31]\n\tv_exp_f32 v53, v53\n\tv_exp_f32 v54, v54\n\tv_exp_f32 v55, v55\n\tv_exp_f32 v56, v56\n\tv_exp_f32 v57, v57\n\t" \
    "v_exp_f32 v58, v58\n\tv_smfmac_f32_32x32x32_bf16 v[0:15], v[84:87], v[48:55], v92\n\tv_exp_f32 v59, v59\n\tv_exp_f32 v60, v60\n\tv_exp_f32 v61, v61\n\tv_exp_f32 v62, v62\n\t" \
    "v_exp_f32 v63, v63\n\tv_exp_f32 v32, v32\n\tv_exp_f32 v33, v33\n\tv_smfmac_f32_32x32x32_bf16 v[0:15], v[88:91], v[56:63], v92\n\t" \
    "global_load_lds_dwordx4 %[l16], s[86:87] offset:2048 sc1\n\tds_read_b128 v[68:71], v93 offset:4096\n\tds_read_b128 v[84:87], v94 offset:3072\n\t" \
    "ds_read_b128 v[88:91], v94 offset:3584\n\tv_exp_f32 v34, v34\n\tv_exp_f32 v35, v35\n\tv_exp_f32 v36, v36\n\ts_waitcnt lgkmcnt(3)\n\t" \
    "v_mfma_f32_32x32x16_bf16 v[48:63], v[72:75], v[64:67], v[16:31]\n\tv_exp_f32 v37, v37\n\tv_exp_f32 v38, v38\n\tv_exp_f32 v39, v39\n\tv_exp_f32 v40, v40\n\tv_exp_f32 v41, v41\n\t" \
    "v_exp_f32 v42, v42\n\tv_smfmac_f32_32x32x32_bf16 v[0:15], v[76:79], v[32:39], v92\n\tv_exp_f32 v43, v43\n\tv_exp_f32 v44, v44\n\tv_exp_f32 v45, v45\n\tv_exp_f32 v46, v46\n\t" \
    "v_exp_f32 v47, v47\n\tv_exp_f32 v48, v48\n\tv_exp_f32 v49, v49\n\tv_smfmac_f32_32x32x32_bf16 v[0:15], v[80:83], v[40:47], v92\n\t" \
    "global_load_lds_dwordx4 %[l16], s[86:87] offset:3072 sc1\n\tv_add_u32 v94, v94, v95\n\tds_read_b128 v[72:75], v93 offset:5120\n\tds_read_b128 v[76:79], v94 offset:0\n\t" \
    "ds_read_b128 v[80:83], v94 offset:512\n\tv_exp_f32 v50, v50\n\tv_exp_f32 v51, v51\n\tv_exp_f32 v52, v52\n\ts_waitcnt lgkmcnt(3)\n\t" \
    "v_mfma_f32_32x32x16_bf16 v[32:47], v[68:71], v[64:67], v[16:31]\n\tv_exp_f32 v53, v53\n\tv_exp_f32 v54, v54\n\tv_exp_f32 v55, v55\n\tv_exp_f32 v56, v56\n\tv_exp_f32 v57, v57\n\t" \
    "v_exp_f32 v58, v58\n\tv_smfmac_f32_32x32x32_bf16 v[0:15], v[84:87], v[48:55], v92\n\tv_exp_f32 v59, v59\n\tv_exp_f32 v60, v60\n\tv_exp_f32 v61, v61\n\tv_exp_f32 v62, v62\n\t" \
    "v_exp_f32 v63, v63\n\tv_add_u32 v93, 0x1000, v93\n\ts_setprio 2\n\tv_exp_f32 v32, v32\n\tv_exp_f32 v33, v33\n\tv_smfmac_f32_32x32x32_bf16 v[0:15], v[88:91], v[56:63], v92\n\t" \
    "buffer_load_dwordx4 v[224:227], %[l16], %[rs], %[foo] offen offset:0 sc1\n\tds_read_b128 v[68:71], v93 offset:2048\n\tds_read_b128 v[84:87], v94 offset:1024\n\t" \
    "ds_read_b128 v[88:91], v94 offset:1536\n\tv_exp_f32 v34, v34\n\tv_exp_f32 v35, v35\n\tv_exp_f32 v36, v36\n\ts_waitcnt lgkmcnt(3)\n\t" \
    "v_mfma_f32_32x32x16_bf16 v[48:63], v[72:75], v[64:67], v[16:31]\n\tv_exp_f32 v37, v37\n\tv_exp_f32 v38, v38\n\tv_exp_f32 v39, v39\n\tv_exp_f32 v40, v40\n\tv_exp_f32 v41, v41\n\t" \
    "v_exp_f32 v42, v42\n\tv_smfmac_f32_32x32x32_bf16 v[0:15], v[76:79], v[32:39], v92\n\tv_exp_f32 v43, v43\n\tv_exp_f32 v44, v44\n\tv_exp_f32 v45, v45\n\tv_exp_f32 v46, v46\n\t" \
    "v_exp_f32 v47, v47\n\tv_exp_f32 v48, v48\n\tv_exp_f32 v49, v49\n\tv_smfmac_f32_32x32x32_bf16 v[0:15], v[80:83], v[40:47], v92\n\t" \
    "buffer_load_dwordx4 v[228:231], %[l16], %[rs], %[foo] offen offset:1024 sc1\n\tds_read_b128 v[72:75], v93 offset:3072\n\tds_read_b128 v[76:79], v94 offset:2048\n\t" \
    "ds_read_b128 v[80:83], v94 offset:2560\n\tv_exp_f32 v50, v50\n\tv_exp_f32 v51, v51\n\tv_exp_f32 v52, v52\n\ts_waitcnt lgkmcnt(3)\n\t" \
    "v_mfma_f32_32x32x16_bf16 v[32:47], v[68:71], v[64:67], v[16:31]\n\tv_exp_f32 v53, v53\n\tv_exp_f32 v54, v54\n\tv_exp_f32 v55, v55\n\tv_exp_f32 v56, v56\n\tv_exp_f32 v57, v57\n\t" \
    "v_exp_f32 v58, v58\n\tv_smfmac_f32_32x32x32_bf16 v[0:15], v[84:87], v[48:55], v92\n\tv_exp_f32 v59, v59\n\tv_exp_f32 v60, v60\n\tv_exp_f32 v61, v61\n\tv_exp_f32 v62, v62\n\t" \
    "v_exp_f32 v63, v63\n\ts_setprio 1\n\tv_exp_f32 v32, v32\n\tv_exp_f32 v33, v33\n\tv_smfmac_f32_32x32x32_bf16 v[0:15], v[88:91], v[56:63], v92\n\t" \
    "buffer_load_dwordx4 v[232:235], %[l16], %[rs], %[foo] offen offset:2048 sc1\n\tds_read_b128 v[68:71], v93 offset:4096\n\tds_read_b128 v[84:87], v94 offset:3072\n\t" \
    "ds_read_b128 v[88:91], v94 offset:3584\n\tv_exp_f32 v34, v34\n\tv_exp_f32 v35, v35\n\tv_exp_f32 v36, v36\n\ts_waitcnt lgkmcnt(3)\n\t" \
    "v_mfma_f32_32x32x16_bf16 v[48:63], v[72:75], v[64:67], v[16:31]\n\tv_exp_f32 v37, v37\n\tv_exp_f32 v38, v38\n\tv_exp_f32 v39, v39\n\tv_exp_f32 v40, v40\n\tv_exp_f32 v41, v41\n\t" \
    "v_exp_f32 v42, v42\n\tv_smfmac_f32_32x32x32_bf16 v[0:15], v[76:79], v[32:39], v92\n\tv_exp_f32 v43, v43\n\tv_exp_f32 v44, v44\n\tv_exp_f32 v45, v45\n\tv_exp_f32 v46, v46\n\t" \
    "v_exp_f32 v47, v47\n\tv_exp_f32 v48, v48\n\tv_exp_f32 v49, v49\n\tv_smfmac_f32_32x32x32_bf16 v[0:15], v[80:83], v[40:47], v92\n\t" \
    "buffer_load_dwordx4 v[236:239], %[l16], %[rs], %[foo] offen offset:3072 sc1\n\tv_add_u32 v94, v94, v95\n\tds_read_b128 v[72:75], v93 offset:5120\n\t" \
    "ds_read_b128 v[76:79], v94 offset:0\n\tds_read_b128 v[80:83], v94 offset:512\n\tv_exp_f32 v50, v50\n\tv_exp_f32 v51, v51\n\tv_exp_f32 v52, v52\n\ts_waitcnt lgkmcnt(3)\n\t" \
    "v_mfma_f32_32x32x16_bf16 v[32:47], v[68:71], v[64:67], v[16:31]\n\tv_exp_f32 v53, v53\n\tv_exp_f32 v54, v54\n\tv_exp_f32 v55, v55\n\tv_exp_f32 v56, v56\n\tv_exp_f32 v57, v57\n\t" \
    "v_exp_f32 v58, v58\n\tv_smfmac_f32_32x32x32_bf16 v[0:15], v[84:87], v[48:55], v92\n\tv_exp_f32 v59, v59\n\tv_exp_f32 v60, v60\n\tv_exp_f32 v61, v61\n\tv_exp_f32 v62, v62\n\t" \
    "v_exp_f32 v63, v63\n\tv_add_u32 v93, 0x1000, v93\n\ts_nop 1\n\tv_smfmac_f32_32x32x32_bf16 v[0:15], v[88:91], v[56:63], v92\n\ts_waitcnt lgkmcnt(0)\n\ts_nop 7\n\ts_nop 7\n\t" \
    "s_mov_b32 m0, s88\n\ts_setprio 0\n\ts_waitcnt vmcnt(4)\n\t"
#define ATT_CLOB_A "v16", "v17", "v18", "v19", "v20", "v21", "v22", "v23", "v24", "v25", "v26", "v27", "v28", "v29", "v30", "v31", "v32", "v33", "v34", "v35", "v36", "v37", "v38", "v39", "v40", "v41", "v42", "v43", "v44", "v45", "v46", "v47", "v48", "v49", "v50", "v51", "v52", "v53", "v54", "v55", "v56", "v57", "v58", "v59", "v60", "v61", "v62", "v63", "v68", "v69", "v70", "v71", "v72", "v73", "v74", "v75", "v76", "v77", "v78", "v79", "v80", "v81", "v82", "v83", "v84", "v85", "v86", "v87", "v88", "v89", "v90", "v91", "v92", "v93", "v94", "v95", "v96", "v97", "v98", "v99", "v100", "v101", "v102", "v103", "v104", "v105", "v106", "v107", "v108", "v109", "v110", "v111", "v112", "v113", "v114", "v115", "v116", "v117", "s86", "s87", "s88", "s89", "s90", "s91", "vcc", "scc", "memory"
#define ATT_CLOB_B "v16", "v17", "v18", "v19", "v20", "v21", "v22", "v23", "v24", "v25", "v26", "v27", "v28", "v29", "v30", "v31", "v32", "v33", "v34", "v35", "v36", "v37", "v38", "v39", "v40", "v41", "v42", "v43", "v44", "v45", "v46", "v47", "v48", "v49", "v50", "v51", "v52", "v53", "v54", "v55", "v56", "v57", "v58", "v59", "v60", "v61", "v62", "v63", "v68", "v69", "v70", "v71", "v72", "v73", "v74", "v75", "v76", "v77", "v78", "v79", "v80", "v81", "v82", "v83", "v84", "v85", "v86", "v87", "v88", "v89", "v90", "v91", "v92", "v93", "v94", "v95", "s86", "s87", "s88", "s89", "s90", "s91", "vcc", "scc", "memory"

typedef __attribute__((ext_vector_type(32))) unsigned u32x32;
typedef __attribute__((ext_vector_type(16))) unsigned u32x16;
#define SUB4(v, i) __builtin_shufflevector(v, v, 4 * (i), 4 * (i) + 1, 4 * (i) + 2, 4 * (i) + 3)
#define PRIO(n) __builtin_amdgcn_s_setprio(n)
#define BAR() asm volatile("s_waitcnt lgkmcnt(0)\n\ts_barrier" ::: "memory")

template <int N>
__device__ __forceinline__ void load_frags(u32x4 (&f)[N], rsrc_t rs, unsigned voff, unsigned soff) {
#pragma unroll
    for (int i = 0; i < N; ++i) f[i] = ld_sc1(rs, voff + (i & 3) * 1024, soff + (i >> 2) * 4096);
}
template <int N>
__device__ __forceinline__ bool pending(const u32x4 (&f)[N]) {
    unsigned m = 0;
#pragma unroll
    for (int i = 0; i < N; ++i) m = umax4(m, f[i]);
    return __any(m == SENT);
}
template <int N>
__device__ __forceinline__ void settle_frags(u32x4 (&f)[N], rsrc_t rs, unsigned voff, unsigned soff) {
    if (pending<N>(f)) {
        int spin = 0;
#pragma nounroll
        do {
            __builtin_amdgcn_s_sleep(2);
            asm volatile("" ::: "memory");
            load_frags<N>(f, rs, voff, soff);
            ++spin;
        } while (pending<N>(f) & (spin < SPIN_MAX));
    }
}
#define PIN2(a, b) asm volatile("" : "+v"(a), "+v"(b))
__device__ __forceinline__ void put_partials(unsigned base0, unsigned base1, const f32x16& c0, const f32x16& c1) {
#pragma unroll
    for (int g = 0; g < 4; ++g) {
        *(LAS f32x4*)(base0 + g * 1024) = f32x4{c0[4 * g], c0[4 * g + 1], c0[4 * g + 2], c0[4 * g + 3]};
        *(LAS f32x4*)(base1 + g * 1024) = f32x4{c1[4 * g], c1[4 * g + 1], c1[4 * g + 2], c1[4 * g + 3]};
    }
}

__device__ __forceinline__ void cnt_wait(const unsigned* p) {
    unsigned c = __hip_atomic_load(p, __ATOMIC_RELAXED, __HIP_MEMORY_SCOPE_AGENT);
    if (!__all(c >= 32u)) {
        int spin = 0;
#pragma nounroll
        do {
            __builtin_amdgcn_s_sleep(8);
            c = __hip_atomic_load(p, __ATOMIC_RELAXED, __HIP_MEMORY_SCOPE_AGENT);
            ++spin;
        } while (!__all(c >= 32u) & (spin < SPIN_MAX));
    }
    asm volatile("" ::: "memory");
}
template <int N>
__device__ __forceinline__ void probe(rsrc_t rs, unsigned soff) {
    const unsigned voff = ((threadIdx.x & 63) % N) * 1024 + 1020;
    unsigned c = __builtin_amdgcn_raw_buffer_load_b32(rs, voff, soff, 16);
    if (__any(c == SENT)) {
        int spin = 0;
#pragma nounroll
        do {
            __builtin_amdgcn_s_sleep(PROBE_SLEEP);
            asm volatile("" ::: "memory");
            c = __builtin_amdgcn_raw_buffer_load_b32(rs, voff, soff, 16);
            ++spin;
        } while (__any(c == SENT) & (spin < SPIN_MAX));
    }
    asm volatile("" ::: "memory");
}
__device__ __forceinline__ float sum_rows16(float v) {
    auto t = __builtin_amdgcn_permlane16_swap(__float_as_uint(v), __float_as_uint(v), false, false);
    return __uint_as_float(t[0]) + __uint_as_float(t[1]);
}

__global__ __launch_bounds__(512, 2) void k2_mega(char* __restrict__ ws, const unsigned* __restrict__ nq, const u16* __restrict__ x, const u16* __restrict__ wo,
                                                  const u16* __restrict__ wg, const u16* __restrict__ wu, const u16* __restrict__ wd, u16* __restrict__ out) {
    __shared__ __attribute__((aligned(16))) char lds[L_END];
    const unsigned lds0 = (unsigned)(uintptr_t)lds;
    const int b = blockIdx.x, xc = b & 7, bi = b >> 3;
    const int head = xc * 4 + (bi & 3), qb = bi >> 2;
    const int mi = bi & 3, ni = bi >> 2;
    const int rtP = ((xc >> 1) * 2 + ((bi >> 1) & 1)) * 8 + 2 * (bi & 1), fs = (xc & 1) * 8 + (bi >> 2);
    const int lane = threadIdx.x & 63, w = rfl(threadIdx.x >> 6), r = lane & 31, h = lane >> 5;
    const int kvh = w >> 2, qi = w & 3;
    const unsigned l16 = lane * 16;
    STAMP_AT(ws, 1, 0);

    u32x4 qA, qB; unsigned nk;
    const char* kvsrc = ws + ((w & 1) ? WS_V : WS_K) + (size_t)head * 65536 + ((w >> 1) & 1) * 32768 + (w >> 2) * 1024;
    const unsigned kvdst = lds0 + ((w & 1) ? L_V : L_K) + ((w >> 1) & 1) * 32768 + (w >> 2) * 1024;
    {
        const char* qp = ws + WS_Q + ((size_t)head * 64 + qb * 8 + qi) * 1024;
#pragma unroll
        for (int i = 0; i < 3; ++i) glds16(kvsrc + i * 2048, l16, kvdst + i * 2048);
        asm volatile("global_load_dwordx4 %0, %3, %4\n\tglobal_load_dwordx4 %1, %3, %5\n\tglobal_load_dword %2, %6, %7"
                     : "=&v"(qA), "=&v"(qB), "=&v"(nk) : "v"(l16), "s"(qp), "s"(qp + 4096), "v"(lane * 4), "s"(ws + WS_NK + head * 256) : "memory");
    }
    const rsrc_t rs = __builtin_amdgcn_make_buffer_rsrc(ws, 0, (int)WS_END, 0x00020000);
    { const unsigned one = (((threadIdx.x >> 1) & 11) == 0) ? 0x3f803f80u : 0u; *(LAS u32x2*)(lds0 + L_ONE + threadIdx.x * 8) = u32x2{one, one}; }
    if (threadIdx.x < 16) *(LAS unsigned*)(lds0 + L_FLAG + threadIdx.x * 4) = 0u;
    unsigned qm = 0;
#pragma unroll
    for (int i = 0; i < 8; ++i) qm = max(qm, nq[head * 64 + qb * 8 + i]);
    const unsigned ka0 = lds0 + L_K + kvh * 32768 + l16;
    const unsigned va0 = (r < 16) ? (lds0 + L_V + kvh * 32768 + h * 256 + r * 16) : (lds0 + L_ONE + h * 256 + (r & 15) * 16);
    const unsigned vs0 = (r < 16) ? 4096u : 0u;
#pragma unroll
    for (int i = 3; i < 8; ++i) glds16(kvsrc + i * 2048, l16, kvdst + i * 2048);
    asm volatile("s_waitcnt vmcnt(5) lgkmcnt(0)\n\ts_barrier" : "+v"(qA), "+v"(qB), "+v"(nk) :: "memory");
    STAMP_AT(ws, 1, 1);
    float mA = 0.f, mB = 0.f;
    if (!__all(__uint_as_float(nk) * __uint_as_float(qm) <= 1600.f)) {
#pragma unroll
        for (int i = 8; i < 16; ++i) glds16(kvsrc + i * 2048, l16, kvdst + i * 2048);
        asm volatile("s_waitcnt vmcnt(0)\n\ts_barrier" ::: "memory");
        mA = -3.0e38f; mB = -3.0e38f;
        for (int t = 0; t < 64; ++t) {
            const u32x4 kf = lds_ld128(lds0 + L_K + t * 1024 + l16);
            mA = fmaxf(mA, max16(mfma32(kf, qA, f32x16{}))); mB = fmaxf(mB, max16(mfma32(kf, qB, f32x16{})));
        }
        mA = max_halves(mA); mB = max_halves(mB);
        BAR();
    }
    const int rtA = xc * 8 + mi, rtB = rtA + 4;
    const int fct = (w >> 1) & 1, fj = w & 1;
    const int frt = (w < 4) ? rtA : rtB, fks = ni * 4 + fct * 2 + fj;
    const unsigned apo = (unsigned)WS_A + (rtP * 112 + fs * 7 + w) * 1024;
    f32x16 oacc;
    {
        const int gw = w * 256 + b;
        const bool wact = gw < 1472, wgu = gw >= 128 && gw < 1024;
        const char *w0, *w2; unsigned pitch, d0, d2;
        if (wgu) { const int idx = gw - 128, cp = idx >> 4, kg = idx & 15; pitch = FF * 2;
            w0 = (const char*)wg + (size_t)(32 * kg) * (FF * 2) + 64 * cp; w2 = (const char*)wu + (size_t)(32 * kg) * (FF * 2) + 64 * cp;
            d0 = (unsigned)WS_WGU + (2 * cp * 32 + 2 * kg) * 1024; d2 = d0 + 32768;
        } else { const bool iso = gw < 128; const int idx = iso ? gw : (wact ? gw - 1024 : 0), ct = iso ? idx >> 3 : idx / 28, kg = iso ? idx & 7 : idx % 28; pitch = 1024;
            w0 = (const char*)(iso ? wo : wd) + (size_t)(64 * kg) * 1024 + 64 * ct; w2 = w0 + 32768;
            d0 = (iso ? (unsigned)WS_WO + ct * 32 * 1024 : (unsigned)WS_WD + ct * 112 * 1024) + 4 * kg * 1024; d2 = d0 + 2048;
        }
        const unsigned wv0 = (lane >> 2) * pitch + (lane & 3) * 16, wv1 = wv0 + 16 * pitch;
        const unsigned wld = lds0 + L_R + (wact ? w : 0) * 4096;
        const unsigned t0 = wld + (4 * h + ((lane & 15) >> 2)) * 64 + (lane & 3) * 8 + ((lane >> 4) & 1) * (wgu ? 2048 : 32), t2 = t0 + (wgu ? 32 : 2048);
        const unsigned flg = rfl((wact ? 1 : 0) | (w == 0 ? 2 : 0) | (w < 7 ? 4 : 0));
        const unsigned hpo = (unsigned)WS_H + (frt * 32 + fks) * 1024;
        asm volatile(ATT_STAGE_A : "=&{v[0:15]}"(oacc)
                     : "{v[64:67]}"(qA), [negm] "v"(-mA), [ka] "v"(ka0), [va] "v"(va0), [vs] "v"(vs0), [l16] "v"(l16), [kvs] "s"(kvsrc + 16384), [kvd] "s"(kvdst + 16384),
                       [w0] "s"(w0), [w2] "s"(w2), [wv0] "v"(wv0), [wv1] "v"(wv1), [wld] "s"(wld), [flg] "s"(flg), [rs] "s"(rs),
                       [po] "s"((unsigned)WS_O + ((qb * 8 + w) * 32 + head) * 1024), [ph] "s"(hpo), [pa] "s"(w < 7 ? apo : hpo), [t0] "v"(t0), [t2] "v"(t2), [d0] "s"(d0), [d2] "s"(d2),
                       [cso] "s"((unsigned)WS_CNT + (bi & 7) * 128)
                     : ATT_CLOB_A);
    }
    const unsigned xch = lds0 + L_R + qi * 4096 + lane * 4;
    auto finish_o = [&](int rt) {
        float o[9];
#pragma unroll
        for (int i = 0; i < 9; ++i) o[i] = oacc[i] + *(const LAS float*)(xch + i * 256);
        const float rl = __builtin_amdgcn_rcpf(o[8]);
        u32x4 t;
#pragma unroll
        for (int e = 0; e < 4; ++e) t[e] = clean(pk_bf16(o[2 * e] * rl, o[2 * e + 1] * rl));
        st_sc1(rs, l16, (unsigned)WS_O + (rt * 32 + head) * 1024, t);
    };
    if (kvh == 0) {
#pragma unroll
        for (int i = 0; i < 9; ++i) *(LAS float*)(xch + i * 256) = oacc[i];
    }
    STAMP_AT(ws, 1, 7);
    if (w == 0) cnt_wait((const unsigned*)(ws + WS_CNT + xc * 1024 + (lane & 7) * 128));
    BAR();
    if (kvh == 1) finish_o(qb * 8 + qi);
    STAMP_AT(ws, 1, 8);
    u32x32 wq0, wq1, wq2, wq3; u32x16 foq;
    asm volatile(ATT_STAGE_B : "=&{v[0:15]}"(oacc), "=&{v[96:127]}"(wq0), "=&{v[128:159]}"(wq1), "=&{v[160:191]}"(wq2), "=&{v[192:223]}"(wq3), "=&{v[224:239]}"(foq)
                 : "{v[64:67]}"(qB), [negm] "v"(-mB), [ka] "v"(ka0), [va] "v"(va0), [vs] "v"(vs0), [l16] "v"(l16), [rs] "s"(rs), [flg] "s"(w < 7 ? 4u : 0u),
                   [wbo] "s"((unsigned)WS_WGU + (fs * 7 + w) * 32 * 1024), [wos] "s"(ws + WS_WO + (size_t)(ni * 64 + 4 * w) * 1024), [wod] "s"(lds0 + piece(4 * w)),
                   [foo] "s"((unsigned)WS_O + (rtA * 32 + 4 * w) * 1024), [wds] "s"(ws + WS_WD + (size_t)(bi * 56 + w * 7) * 1024), [snk] "s"(lds0 + L_CS),
                   [xs] "s"((const char*)x + (size_t)(frt * 32 + qi * 8) * 1024 + ni * 128), [xv] "v"((lane >> 3) * 1024 + (lane & 7) * 16), [xd] "s"(lds0 + L_R + 16384 + w * 1024)
                 : ATT_CLOB_B);
    if (kvh == 0) {
#pragma unroll
        for (int i = 0; i < 9; ++i) *(LAS float*)(xch + i * 256) = oacc[i];
    }
    asm volatile("s_waitcnt vmcnt(0) lgkmcnt(0)\n\ts_barrier" : "+{v[224:239]}"(foq) :: "memory");
    u32x4 fo[4] = {SUB4(foq, 0), SUB4(foq, 1), SUB4(foq, 2), SUB4(foq, 3)};
    u32x4 wb[32];
#define WBX(q, n) wb[8 * n] = SUB4(q, 0); wb[8 * n + 1] = SUB4(q, 1); wb[8 * n + 2] = SUB4(q, 2); wb[8 * n + 3] = SUB4(q, 3); \
                  wb[8 * n + 4] = SUB4(q, 4); wb[8 * n + 5] = SUB4(q, 5); wb[8 * n + 6] = SUB4(q, 6); wb[8 * n + 7] = SUB4(q, 7);
    WBX(wq0, 0) WBX(wq1, 1) WBX(wq2, 2) WBX(wq3, 3)
    STAMP_AT(ws, 1, 10);
    if (kvh == 1) finish_o(qb * 8 + 4 + qi);
    u32x4 hres = {};
    auto gather8 = [&](unsigned off_lo, bool upper, float (&s)[8]) {
#pragma unroll
        for (int e = 0; e < 8; ++e) s[e] = 0.f;
#pragma unroll
        for (int v = 0; v < 8; ++v) {
            const unsigned p = upper ? lds0 + (v >> 1) * 32768 + 16384 + (v & 1) * 8192 + fct * 4096 : lds0 + piece(fct * 32 + 4 * v);
            const f32x4 t0 = lds_ldf4(p + off_lo), t1 = lds_ldf4(p + off_lo + 1024);
#pragma unroll
            for (int e = 0; e < 4; ++e) { s[e] += t0[e]; s[4 + e] += t1[e]; }
            if (v == 3) __builtin_amdgcn_sched_barrier(0);
        }
    };
    auto publish_h = [&](const float* s, u32x2 x0, u32x2 x1, int rt, int ks) {
        const u32x4 hv = {clean(pk_bf16(s[0] + bf_lo(x0[0]), s[1] + bf_hi(x0[0]))), clean(pk_bf16(s[2] + bf_lo(x0[1]), s[3] + bf_hi(x0[1]))),
                          clean(pk_bf16(s[4] + bf_lo(x1[0]), s[5] + bf_hi(x1[0]))), clean(pk_bf16(s[6] + bf_lo(x1[1]), s[7] + bf_hi(x1[1])))};
        st_sc1(rs, l16, (unsigned)WS_H + (rt * 32 + ks) * 1024, hv);
        float sx = 0.f, sxx = 0.f;
#pragma unroll
        for (int e = 0; e < 4; ++e) { sx = dot2(hv[e], 0x3f803f80u, sx); sxx = dot2(hv[e], hv[e], sxx); }
        sx = sum_halves(sx); sxx = sum_halves(sxx);
        __builtin_amdgcn_raw_buffer_store_b32(clean(__float_as_uint(h ? sxx : sx)), rs, r * 8 + h * 4, (unsigned)WS_ST + (rt * 32 + ks) * 256, 16);
        return hv;
    };
    const unsigned upo = lds0 + (w >> 1) * 32768 + 16384 + (w & 1) * 8192 + l16;
    const unsigned lo0 = lds0 + piece(4 * w) + l16, lo1 = lds0 + piece(32 + 4 * w) + l16;
    const unsigned xa = lds0 + L_R + 16384 + kvh * 4096 + r * 128 + (w & 3) * 32 + 8 * h;
    const unsigned hao = (unsigned)WS_H + (rtP * 32 + 8 * w) * 1024, hbo = hao + 4 * 32 * 1024;
    unsigned lb0 = lds0 + l16, lb1 = lds0 + 65536 + l16;
    PIN2(lb0, lb1);
    {
        u32x4 bw[8];
#pragma unroll
        for (int k = 0; k < 4; ++k) { bw[k] = lds_ld128(lo0 + k * 1024); bw[4 + k] = lds_ld128(lo1 + k * 1024); }
        settle_frags<4>(fo, rs, l16, (unsigned)WS_O + (rtA * 32 + 4 * w) * 1024);
        STAMP_AT(ws, 1, 11);
        f32x16 c0 = {}, c1 = {};
#pragma unroll
        for (int k = 0; k < 4; ++k) { c0 = mfma32(bw[k], fo[k], c0); c1 = mfma32(bw[4 + k], fo[k], c1); }
        put_partials(upo, upo + 4096, c0, c1);
        BAR();
        if (w < 4) { float s[8]; gather8(fj * 2048 + l16, true, s); hres = publish_h(s, *(const LAS u32x2*)xa, *(const LAS u32x2*)(xa + 16), rtA, fks); }
        STAMP_AT(ws, 1, 12);
        const unsigned obo = (unsigned)WS_O + (rtB * 32 + 4 * w) * 1024;
        probe<4>(rs, obo);
        load_frags<4>(fo, rs, l16, obo);
        STAMP_AT(ws, 1, 13);
        if (w < 7) {
            float c4[4] = {0.f, 0.f, 0.f, 0.f};
#pragma unroll
            for (int k = 0; k < 32; ++k)
#pragma unroll
                for (int e = 0; e < 4; ++e) c4[e] = dot2(wb[k][e], 0x3f803f80u, c4[e]);
            const float t = sum_halves((c4[0] + c4[1]) + (c4[2] + c4[3]));
            if (h == 0) *(LAS float*)(lds0 + L_CS + (w * 32 + r) * 4) = t;
        }
#pragma unroll
        for (int k = 0; k < 4; ++k) { bw[k] = lds_ld128(lo0 + k * 1024); bw[4 + k] = lds_ld128(lo1 + k * 1024); }
        settle_frags<4>(fo, rs, l16, obo);
        STAMP_AT(ws, 1, 14);
        c0 = f32x16{}; c1 = f32x16{};
#pragma unroll
        for (int k = 0; k < 4; ++k) { c0 = mfma32(bw[k], fo[k], c0); c1 = mfma32(bw[4 + k], fo[k], c1); }
        put_partials(lo0, lo1, c0, c1);
        u32x4 fh[8];
        probe<8>(rs, hao);
        load_frags<8>(fh, rs, l16, hao);
        BAR();
        if (w >= 4) { float s[8]; gather8(fj * 2048 + l16, false, s); hres = publish_h(s, *(const LAS u32x2*)xa, *(const LAS u32x2*)(xa + 16), rtB, fks); }
        settle_frags<8>(fh, rs, l16, hao);
        STAMP_AT(ws, 1, 15);
#pragma unroll
        for (int i = 0; i < 8; ++i) *(LAS u32x4*)(lb0 + piece(8 * w + i) + 16384) = fh[i];
        BAR();
    }
    STAMP_AT(ws, 1, 16);
    u32x4 wdv[28];
    const unsigned wdo = (unsigned)WS_WD + (ni * 2 * 112 + 14 * w) * 1024;
    u32x4 fa[14];
    const unsigned aoA = (unsigned)WS_A + (rtA * 112 + 14 * w) * 1024, aoB = (unsigned)WS_A + (rtB * 112 + 14 * w) * 1024;
    if (w == 7) {
        u32x4 sp[16];
        auto stats = [&](int half) {
            settle_frags<16>(sp, rs, l16, (unsigned)WS_ST + (rtP + 4 * half) * 8192);
#pragma unroll
            for (int t = 0; t < 2; ++t) {
                float s[4];
#pragma unroll
                for (int e = 0; e < 4; ++e) { s[e] = 0.f;
#pragma unroll
                    for (int i = 0; i < 8; ++i) s[e] += __uint_as_float(sp[t * 8 + i][e]);
                    s[e] = sum_halves(sum_rows16(s[e])); }
                const float mu0 = s[0] * (1.f / DM), v0 = fmaxf(s[1] * (1.f / DM) - mu0 * mu0, 0.f) + LN_EPS;
                const float mu1 = s[2] * (1.f / DM), v1 = fmaxf(s[3] * (1.f / DM) - mu1 * mu1, 0.f) + LN_EPS;
                if (lane < 16) *(LAS f32x4*)(lds0 + L_ST + (half * 2 + t) * 256 + lane * 16) = f32x4{-mu0, v0, -mu1, v1};
            }
        };
        load_frags<16>(sp, rs, l16, (unsigned)WS_ST + rtP * 8192);
        stats(0);
        lds_signal(lds0 + L_FLAG);
        STAMP_AT(ws, 1, 17);
        u32x4 fh[8];
        probe<8>(rs, hbo);
        load_frags<8>(fh, rs, l16, hbo);
        load_frags<16>(sp, rs, l16, (unsigned)WS_ST + (rtP + 4) * 8192);
        settle_frags<8>(fh, rs, l16, hbo);
#pragma unroll
        for (int i = 0; i < 8; ++i) *(LAS u32x4*)(lb1 + piece(24 + i)) = fh[i];
        STAMP_AT(ws, 1, 18);
        stats(1);
        STAMP_AT(ws, 1, 19);
        BAR();
        STAMP_AT(ws, 1, 20);
#pragma unroll
        for (int c = 0; c < 2; ++c)
#pragma unroll
            for (int k = 0; k < 14; ++k) wdv[c * 14 + k] = ld_sc1(rs, l16 + (k & 3) * 1024, wdo + c * 112 * 1024 + (k >> 2) * 4096);
        probe<14>(rs, aoA);
        load_frags<14>(fa, rs, l16, aoA);
    } else {
        constexpr float C1 = -2.0f * 0.7978845608028654f * 1.4426950408889634f, C2 = C1 * 0.044715f;
        f32x2 nmu, k1, k2, vv; u32x4 av;
        auto epi_begin = [&](int slot) {
            const f32x2 st = *(const LAS f32x2*)(lds0 + L_ST + (slot * 32 + r) * 8);
            const float rstd = __builtin_amdgcn_rsqf(st[1]);
            nmu = f32x2{st[0], st[0]}; vv = f32x2{st[1], st[1]};
            const float a1 = C1 * rstd, a2 = C2 * rstd * rstd * rstd;
            k1 = f32x2{a1, a1}; k2 = f32x2{a2, a2};
            PIN2(nmu, vv); PIN2(k1, k2);
        };
        auto epi_pair = [&](const f32x16& acc, int p) {
            const unsigned co = lds0 + L_CS + (w * 32 + 8 * (p >> 1) + 4 * h + 2 * (p & 1)) * 4;
            const f32x2 cg = *(const LAS f32x2*)co, cu = *(const LAS f32x2*)(co + 64);
            const f32x2 g = __builtin_elementwise_fma(nmu, cg, f32x2{acc[2 * p], acc[2 * p + 1]});
            const f32x2 u = __builtin_elementwise_fma(nmu, cu, f32x2{acc[8 + 2 * p], acc[9 + 2 * p]});
            const f32x2 z = g * __builtin_elementwise_fma(g * g, k2, k1);
            const f32x2 e = {__builtin_amdgcn_exp2f(z[0]), __builtin_amdgcn_exp2f(z[1])};
            const f32x2 d = __builtin_elementwise_fma(e, vv, vv);
            const f32x2 ri = {__builtin_amdgcn_rcpf(d[0]), __builtin_amdgcn_rcpf(d[1])};
            const f32x2 o = (g * u) * ri;
            av[p] = clean(pk_bf16(o[0], o[1]));
        };
        f32x16 c0 = {}, c1 = {}, c2 = {}, c3 = {};
#pragma unroll
        for (int k = 0; k < 32; ++k) c0 = mfma32(wb[k], lds_ld128(lb0 + piece(k) + 16384), c0);
        asm volatile("" : "+v"(c0));
        STAMP_AT(ws, 1, 17);
        lds_wait_ge(lds0 + L_FLAG, 1);
        STAMP_AT(ws, 1, 18);
        u32x4 fh[8];
        epi_begin(0);
#pragma unroll
        for (int k = 0; k < 32; ++k) {
            c1 = mfma32(wb[k], lds_ld128(lb1 + piece(k) + 16384), c1);
            if ((k & 7) == 0) epi_pair(c0, k >> 3);
            if (k == 8) load_frags<8>(fh, rs, l16, hbo);
            if ((k & 7) == 7) __builtin_amdgcn_sched_barrier(0);
        }
        st_sc1(rs, l16, apo, av);
        asm volatile("" : "+v"(c1));
        STAMP_AT(ws, 1, 19);
        settle_frags<8>(fh, rs, l16, hbo);
#pragma unroll
        for (int i = 0; i < 8; ++i) *(LAS u32x4*)(lb0 + piece(8 * w + i)) = fh[i];
        BAR();
        STAMP_AT(ws, 1, 20);
        epi_begin(1);
#pragma unroll
        for (int k = 0; k < 32; ++k) {
            c2 = mfma32(wb[k], lds_ld128(lb0 + piece(k)), c2);
            c3 = mfma32(wb[k], lds_ld128(lb1 + piece(k)), c3);
            if (k < 16 && (k & 3) == 0) epi_pair(c1, k >> 2);
            if (k == 16) st_sc1(rs, l16, apo + 112 * 1024, av);
            if (k < 28) wdv[k] = ld_sc1(rs, l16 + ((k % 14) & 3) * 1024, wdo + (k / 14) * 112 * 1024 + ((k % 14) >> 2) * 4096);
            if ((k & 3) == 3) __builtin_amdgcn_sched_barrier(0);
        }
        PIN2(c2, c3);
        STAMP_AT(ws, 1, 21);
        epi_begin(2);
#pragma unroll
        for (int p = 0; p < 4; ++p) epi_pair(c2, p);
        st_sc1(rs, l16, apo + 4 * 112 * 1024, av);
        epi_begin(3);
#pragma unroll
        for (int p = 0; p < 4; ++p) epi_pair(c3, p);
        st_sc1(rs, l16, apo + 5 * 112 * 1024, av);
        load_frags<14>(fa, rs, l16, aoA);
        STAMP_AT(ws, 1, 22);
    }
    {
        settle_frags<14>(fa, rs, l16, aoA);
        STAMP_AT(ws, 1, 24);
        f32x16 c0 = {}, c1 = {};
#pragma unroll
        for (int k = 0; k < 14; ++k) { c0 = mfma32(wdv[k], fa[k], c0); c1 = mfma32(wdv[14 + k], fa[k], c1); }
        PIN2(c0, c1);
        probe<14>(rs, aoB);
        STAMP_AT(ws, 1, 23);
        load_frags<14>(fa, rs, l16, aoB);
        put_partials(upo, upo + 4096, c0, c1);
        STAMP_AT(ws, 1, 25);
        BAR();
        settle_frags<14>(fa, rs, l16, aoB);
        STAMP_AT(ws, 1, 26);
        c0 = f32x16{}; c1 = f32x16{};
#pragma unroll
        for (int k = 0; k < 14; ++k) { c0 = mfma32(wdv[k], fa[k], c0); c1 = mfma32(wdv[14 + k], fa[k], c1); }
        put_partials(lo0, lo1, c0, c1);
        BAR();
        STAMP_AT(ws, 1, 27);
        float s[8];
        if (w < 4) gather8(fj * 2048 + l16, true, s); else gather8(fj * 2048 + l16, false, s);
        unsigned o0 = pk_bf16(s[0] + bf_lo(hres[0]), s[1] + bf_hi(hres[0])), o1 = pk_bf16(s[2] + bf_lo(hres[1]), s[3] + bf_hi(hres[1]));
        unsigned o2 = pk_bf16(s[4] + bf_lo(hres[2]), s[5] + bf_hi(hres[2])), o3 = pk_bf16(s[6] + bf_lo(hres[3]), s[7] + bf_hi(hres[3]));
        auto s0 = __builtin_amdgcn_permlane32_swap(o0, o2, false, false), s1 = __builtin_amdgcn_permlane32_swap(o1, o3, false, false);
        *(u32x4*)(out + (size_t)(frt * 32 + r) * DM + fks * 16 + 8 * h) = u32x4{s0[0], s1[0], s0[1], s1[1]};
    }
    STAMP_AT(ws, 1, 31);
}

extern "C" void kernel_launch(void* const* d_in, const int* in_sizes, int n_in, void* d_out, int out_size, void* d_ws, size_t ws_size,
                              hipStream_t stream) {
    const u16* x = (const u16*)d_in[0];
    const u16* wqkv = (const u16*)d_in[1];
    const u16* wo = (const u16*)d_in[2];
    const u16* wg = (const u16*)d_in[4];
    const u16* wu = (const u16*)d_in[5];
    const u16* wd = (const u16*)d_in[6];
    char* ws = (char*)d_ws;
    k1_qkv<<<256, 768, 0, stream>>>(x, wqkv, ws);
    k2_mega<<<256, 512, 0, stream>>>(ws, (const unsigned*)(ws + WS_NQ), x, wo, wg, wu, wd, (u16*)d_out);
}
```

```cpp
#include <hip/hip_runtime.h>
#include <hip/hip_bf16.h>
#include <stdint.h>

typedef unsigned short u16;
typedef __attribute__((ext_vector_type(4))) unsigned u32x4;
typedef __attribute__((ext_vector_type(2))) unsigned u32x2;
typedef __attribute__((ext_vector_type(8))) __bf16 bf16x8;
typedef __attribute__((ext_vector_type(16))) __bf16 bf16x16;
typedef __attribute__((ext_vector_type(2))) __bf16 bf16x2;
typedef __attribute__((ext_vector_type(16))) float f32x16;
typedef __attribute__((ext_vector_type(8))) float f32x8;
typedef __attribute__((ext_vector_type(4))) float f32x4;
typedef __attribute__((ext_vector_type(2))) float f32x2;
typedef __attribute__((ext_vector_type(4))) short s16x4;
#define LAS __attribute__((address_space(3)))

constexpr int SEQ = 2048, DM = 512, NH = 32, FF = 1792;
constexpr float LN_EPS = 1e-5f;
constexpr float QSCALE = 0.25f * 1.4426950408889634f;

constexpr size_t WS_Q = 0;
constexpr size_t WS_K = WS_Q + (2u << 20);
constexpr size_t WS_V = WS_K + (2u << 20);
constexpr size_t WS_O = WS_V + (2u << 20);
constexpr size_t WS_H = WS_O + (2u << 20);
constexpr size_t WS_A = WS_H + (2u << 20);
constexpr size_t WS_NQ = WS_A + (size_t)64 * 112 * 1024;
constexpr size_t WS_NK = WS_NQ + 32 * 64 * 4;
constexpr size_t WS_ST = WS_NK + 32 * 64 * 4;
constexpr size_t WS_CNT = WS_ST + (size_t)64 * 8192;
constexpr size_t WS_END = WS_CNT + 64 * 128;

__device__ __forceinline__ f32x16 mfma32(u32x4 a, u32x4 b, f32x16 c) {
    return __builtin_amdgcn_mfma_f32_32x32x16_bf16(__builtin_bit_cast(bf16x8, a), __builtin_bit_cast(bf16x8, b), c, 0, 0, 0);
}
__device__ __forceinline__ unsigned pk_bf16(float lo, float hi) {
    f32x2 v = {lo, hi};
    return __builtin_bit_cast(unsigned, __builtin_convertvector(v, bf16x2));
}
__device__ __forceinline__ float bf_lo(unsigned u) { return __uint_as_float(u << 16); }
__device__ __forceinline__ float bf_hi(unsigned u) { return __uint_as_float(u & 0xffff0000u); }
__device__ __forceinline__ float dot2(unsigned a, unsigned b, float c) {
    return __builtin_amdgcn_fdot2_f32_bf16(__builtin_bit_cast(bf16x2, a), __builtin_bit_cast(bf16x2, b), c, false);
}
__device__ __forceinline__ float xor32(float v) {
    auto r = __builtin_amdgcn_permlane32_swap(__float_as_uint(v), __float_as_uint(v), false, false);
    return (threadIdx.x & 32) ? __uint_as_float(r[0]) : __uint_as_float(r[1]);
}
__device__ __forceinline__ float sum_halves(float v) {
    auto r = __builtin_amdgcn_permlane32_swap(__float_as_uint(v), __float_as_uint(v), false, false);
    return __uint_as_float(r[0]) + __uint_as_float(r[1]);
}
__device__ __forceinline__ float max_halves(float v) {
    auto r = __builtin_amdgcn_permlane32_swap(__float_as_uint(v), __float_as_uint(v), false, false);
    return fmaxf(__uint_as_float(r[0]), __uint_as_float(r[1]));
}
__device__ __forceinline__ void glds16(const void* base, unsigned voff, unsigned lds_dst) {
    unsigned keep;
    asm volatile("s_mov_b32 %0, m0\n\ts_mov_b32 m0, %3\n\ts_nop 0\n\tglobal_load_lds_dwordx4 %1, %2\n\ts_mov_b32 m0, %0"
                 : "=&s"(keep) : "v"(voff), "s"(base), "s"(lds_dst) : "memory");
}
#define WAITV_BAR(n) asm volatile("s_waitcnt vmcnt(" #n ") lgkmcnt(0)\n\ts_barrier" ::: "memory")
#define WAITL_BAR() asm volatile("s_waitcnt lgkmcnt(0)\n\ts_barrier" ::: "memory")
#pragma clang diagnostic ignored "-Wint-to-pointer-cast"
__device__ __forceinline__ u32x4 lds_ld128(unsigned addr) { return *(const LAS u32x4*)addr; }
__device__ __forceinline__ f32x4 lds_ldf4(unsigned addr) { return *(const LAS f32x4*)addr; }
__device__ __forceinline__ u32x2 lds_tr(unsigned addr) {
    return __builtin_bit_cast(u32x2, __builtin_amdgcn_ds_read_tr16_b64_v4i16((LAS s16x4*)addr));
}
#define STAMP_AT(ws, kid, idx) do {} while (0)
__device__ __forceinline__ int rfl(int v) { return __builtin_amdgcn_readfirstlane(v); }
#define SPIN_LOG 17
constexpr int SPIN_MAX = 1 << SPIN_LOG;
__device__ __forceinline__ void lds_wait_ge(unsigned addr, int n) {
    int spin = 0, v;
#pragma nounroll
    do {
        v = rfl(*(volatile LAS int*)addr);
        ++spin;
    } while ((v < n) & (spin < (SPIN_MAX << 6)));
    asm volatile("" ::: "memory");
}
__device__ __forceinline__ void lds_signal(unsigned addr) {
    asm volatile("s_waitcnt lgkmcnt(0)" ::: "memory");
    if ((threadIdx.x & 63) == 0) __hip_atomic_fetch_add((LAS int*)addr, 1, __ATOMIC_RELAXED, __HIP_MEMORY_SCOPE_WORKGROUP);
    asm volatile("" ::: "memory");
}

constexpr int K1_SLOT = 28672, K1_NSLOT = 5, K1_CP = K1_SLOT * K1_NSLOT, K1_CS = K1_CP + 4 * 512, K1_ST = K1_CS + 4 * 512, K1_LDS = K1_ST + 4 * 256;

#define K1_SAUX 0
__device__ __forceinline__ void k1_st(char* ws, u32x4* p, u32x4 v) {
    if (K1_SAUX == 0) *p = v;
    else __builtin_amdgcn_raw_buffer_store_b128(v, __builtin_amdgcn_make_buffer_rsrc(ws, 0, (int)WS_END, 0x00020000), (unsigned)((char*)p - ws), 0, K1_SAUX);
}
template <int NV>
__device__ __forceinline__ void k1_compute(unsigned lds0, int w, int lane, int mt, int nt, char* ws) {
    const int r = lane & 31, h = lane >> 5;
    f32x16 acc[3]; f32x4 cs[3];
#pragma unroll
    for (int j = 0; j < 3; ++j) { acc[j] = f32x16{}; cs[j] = f32x4{}; }
    const unsigned one = ((lane & 15) == ((lane >> 4) & 1)) ? 0x3f803f80u : 0u;
    const u32x4 sel = {one, one, one, one};
    const unsigned boff = 16384 + (8 * h + ((lane & 15) >> 2)) * 192 + (((lane >> 4) & 1) * 16 + (lane & 3) * 4) * 2;
    u32x4 xa[4] = {}, wf[4][3] = {};
    auto rd = [&](unsigned sb, int s) {
        const int sr = (s + w) & 3;
        xa[s] = lds_ld128(sb + (32 * w + r) * 128 + (((2 * sr + h) ^ ((r >> 1) & 7)) * 16));
#pragma unroll
        for (int j = 0; j < 3; ++j) {
            const u32x2 lo = lds_tr(sb + boff + sr * 16 * 192 + j * 64), hi = lds_tr(sb + boff + sr * 16 * 192 + 4 * 192 + j * 64);
            wf[s][j] = u32x4{lo[0], lo[1], hi[0], hi[1]};
        }
    };
    auto mm = [&](int s) {
#pragma unroll
        for (int j = 0; j < 3; ++j) acc[j] = (j >= 3 - NV) ? mfma32(xa[s], wf[s][j], acc[j]) : mfma32(wf[s][j], xa[s], acc[j]);
    };
    int slot = 0;
#pragma nounroll
    for (int c = 0; c < 8; ++c) {
        WAITL_BAR();
        if (c == 0 || c == 4) STAMP_AT(ws, 0, 1 + c);
        const unsigned sb = lds0 + slot * K1_SLOT;
        rd(sb, 0); rd(sb, 1);
        mm(2); mm(3);
#pragma unroll
        for (int g = 0; g < 2; ++g) { __builtin_amdgcn_sched_group_barrier(0x008, 1, 0); __builtin_amdgcn_sched_group_barrier(0x100, 3, 0); }
#pragma unroll
        for (int g = 0; g < 4; ++g) { __builtin_amdgcn_sched_group_barrier(0x008, 1, 0); __builtin_amdgcn_sched_group_barrier(0x100, 2, 0); }
        __builtin_amdgcn_sched_barrier(0);
        rd(sb, 2); rd(sb, 3);
        mm(0); mm(1);
#pragma unroll
        for (int j = 0; j < 3; ++j) cs[j] = __builtin_amdgcn_mfma_f32_16x16x32_bf16(__builtin_bit_cast(bf16x8, sel), __builtin_bit_cast(bf16x8, wf[0][j]), cs[j], 0, 0, 0);
#pragma unroll
        for (int g = 0; g < 7; ++g) { __builtin_amdgcn_sched_group_barrier(0x008, 1, 1); __builtin_amdgcn_sched_group_barrier(0x100, 2, 1); }
        __builtin_amdgcn_sched_barrier(0);
        slot = (slot == K1_NSLOT - 1) ? 0 : slot + 1;
    }
    mm(2); mm(3);
    STAMP_AT(ws, 0, 10);
    if (lane < 16) {
#pragma unroll
        for (int j = 0; j < 3; ++j) { *(LAS float*)(lds0 + K1_CP + w * 512 + (32 * j + lane) * 4) = cs[j][0]; *(LAS float*)(lds0 + K1_CP + w * 512 + (32 * j + 16 + lane) * 4) = cs[j][1]; }
    }
    WAITL_BAR();
    STAMP_AT(ws, 0, 11);
    const unsigned cst = lds0 + K1_CS + w * 512;
    {
        f32x2 t = *(const LAS f32x2*)(lds0 + K1_CP + lane * 8);
#pragma unroll
        for (int p = 1; p < 4; ++p) t += *(const LAS f32x2*)(lds0 + K1_CP + p * 512 + lane * 8);
        *(LAS f32x2*)(cst + lane * 8) = t;
        asm volatile("s_waitcnt lgkmcnt(0)" ::: "memory");
    }
    const float rstd = *(const LAS float*)(lds0 + K1_ST + w * 256 + r * 4), nmr = *(const LAS float*)(lds0 + K1_ST + w * 256 + 128 + r * 4);
    const int tile = mt * 4 + w;
#pragma unroll
    for (int j = 0; j < 3; ++j) {
        const int c0 = nt * 96 + 32 * j, head = (c0 & 511) >> 4;
        if (j >= 3 - NV) {
            const float csv = *(const LAS float*)(cst + (32 * j + r) * 4);
            unsigned o[8];
#pragma unroll
            for (int g = 0; g < 4; ++g) {
                const f32x4 rs = lds_ldf4(lds0 + K1_ST + w * 256 + (8 * g + 4 * h) * 4), nm = lds_ldf4(lds0 + K1_ST + w * 256 + 128 + (8 * g + 4 * h) * 4);
                float v[4];
#pragma unroll
                for (int e = 0; e < 4; ++e) v[e] = fmaf(acc[j][4 * g + e], rs[e], nm[e] * csv);
                o[2 * g] = pk_bf16(v[0], v[1]); o[2 * g + 1] = pk_bf16(v[2], v[3]);
            }
            u32x4* vp = (u32x4*)(ws + WS_V) + (((size_t)(head + (r >> 4)) * 128 + tile * 2) * 2 + h) * 16 + (r & 15);
            k1_st(ws, vp, u32x4{o[0], o[1], o[2], o[3]});
            k1_st(ws, vp + 32, u32x4{o[4], o[5], o[6], o[7]});
        } else {
            const bool isq = c0 < 512;
            const float qs = isq ? QSCALE : 1.0f, sc = rstd * qs, nm = nmr * qs;
            unsigned o[8];
#pragma unroll
            for (int g = 0; g < 4; ++g) {
                const f32x4 c4 = lds_ldf4(cst + (32 * j + 8 * g + 4 * h) * 4);
                float v[4];
#pragma unroll
                for (int e = 0; e < 4; ++e) v[e] = fmaf(acc[j][4 * g + e], sc, nm * c4[e]);
                o[2 * g] = pk_bf16(v[0], v[1]); o[2 * g + 1] = pk_bf16(v[2], v[3]);
            }
            const int rr = isq ? r : ((r & 19) | ((r & 4) << 1) | ((r & 8) >> 1));
            u32x4* qp = (u32x4*)(ws + (isq ? WS_Q : WS_K)) + ((size_t)head * 64 + tile) * 64 + h * 32 + rr;
            k1_st(ws, qp, u32x4{o[0], o[1], o[2], o[3]});
            k1_st(ws, qp + 64 * 64, u32x4{o[4], o[5], o[6], o[7]});
            float n0 = 0.f, n1 = 0.f;
#pragma unroll
            for (int e = 0; e < 4; ++e) { n0 = dot2(o[e], o[e], n0); n1 = dot2(o[4 + e], o[4 + e], n1); }
            n0 = sum_halves(n0); n1 = sum_halves(n1);
            unsigned nn = __float_as_uint(h ? n1 : n0);
            nn = max(nn, (unsigned)__builtin_amdgcn_update_dpp(0, (int)nn, 0x111, 0xf, 0xf, false));
            nn = max(nn, (unsigned)__builtin_amdgcn_update_dpp(0, (int)nn, 0x112, 0xf, 0xf, false));
            nn = max(nn, (unsigned)__builtin_amdgcn_update_dpp(0, (int)nn, 0x114, 0xf, 0xf, false));
            nn = max(nn, (unsigned)__builtin_amdgcn_update_dpp(0, (int)nn, 0x118, 0xf, 0xf, false));
            nn = max(nn, (unsigned)__builtin_amdgcn_update_dpp(0, (int)nn, 0x142, 0xa, 0xf, false));
            if (r == 31) ((unsigned*)(ws + (isq ? WS_NQ : WS_NK)))[(head + h) * 64 + tile] = nn;
        }
    }
}

__global__ __launch_bounds__(768, 3) void k1_qkv(const u16* __restrict__ x, const u16* __restrict__ wqkv, const u16* __restrict__ wo, const u16* __restrict__ wg,
                                                 const u16* __restrict__ wu, const u16* __restrict__ wd, char* __restrict__ ws) {
    __shared__ __attribute__((aligned(16))) char lds[K1_LDS];
    const unsigned lds0 = (unsigned)(uintptr_t)lds;
    const int b = blockIdx.x, xc = b & 7, i = b >> 3;
    const int mt = (xc >> 1) * 4 + (i & 3), nt = (xc & 1) * 8 + (i >> 2);
    const int lane = threadIdx.x & 63, w = rfl(threadIdx.x >> 6), j = w & 3;
    STAMP_AT(ws, 0, 0);
    if (w < 4) {
        if (b == 0 && w == 0) *(unsigned*)(ws + WS_CNT + lane * 128) = 0u;
        if (nt < 10) k1_compute<0>(lds0, w, lane, mt, nt, ws);
        else if (nt == 10) k1_compute<1>(lds0, w, lane, mt, nt, ws);
        else k1_compute<3>(lds0, w, lane, mt, nt, ws);
        STAMP_AT(ws, 0, 31);
    } else if (w < 8) {
        unsigned xo[4], wof[3];
#pragma unroll
        for (int p = 0; p < 4; ++p) { const int row = 8 * (4 * j + p) + (lane >> 3); xo[p] = row * 1024 + (((lane & 7) ^ ((row >> 1) & 7)) * 16); }
#pragma unroll
        for (int p = 0; p < 3; ++p) { const int off = (3 * j + p) * 1024 + lane * 16; wof[p] = (off / 192) * 3072 + (off % 192); }
        const char* xb = (const char*)x + (size_t)mt * 128 * 1024;
        const char* wb = (const char*)wqkv + nt * 192;
#define K1_ROT ((i >> 2) + 2 * (i & 3))
        const int rot = K1_ROT;
        auto issue = [&](int c, int slot) {
            const unsigned sb = lds0 + slot * K1_SLOT;
            const int kc = (c + rot) & 7;
#pragma unroll
            for (int p = 0; p < 4; ++p) glds16(xb + kc * 128, xo[p], sb + (4 * j + p) * 1024);
#pragma unroll
            for (int p = 0; p < 3; ++p) glds16(wb + (size_t)kc * 64 * 3072, wof[p], sb + 16384 + (3 * j + p) * 1024);
        };
        issue(0, 0); issue(1, 1); issue(2, 2);
        WAITV_BAR(14); issue(3, 3); issue(4, 4);
        WAITV_BAR(21); issue(5, 0);
        WAITV_BAR(21); issue(6, 1);
        WAITV_BAR(21); issue(7, 2);
#define K1_PRE 1
        const char* pb = (j == 0 ? (const char*)wg : j == 1 ? (const char*)wu : j == 2 ? (const char*)wd : (const char*)wo) + (size_t)b * (j == 3 ? 2048 : 7168);
        WAITV_BAR(21);
        if (K1_PRE) {
#pragma unroll
            for (int p = 0; p < 7; ++p) glds16(pb + (j == 3 ? (p & 1) : p) * 1024, lane * 16, lds0 + 3 * K1_SLOT + j * 1024);
            WAITV_BAR(21);
            WAITV_BAR(14);
            WAITV_BAR(7);
        } else { WAITV_BAR(14); WAITV_BAR(7); WAITV_BAR(0); }
        asm volatile("s_barrier" ::: "memory");
    } else {
        const int r = lane & 31, h = lane >> 5;
        float s0 = 0.f, s1 = 0.f, q0 = 0.f, q1 = 0.f;
#define K1_PAUX 16
        const __amdgpu_buffer_rsrc_t rs = __builtin_amdgcn_make_buffer_rsrc(ws, 0, (int)WS_END, 0x00020000);
        const u32x4 sv = {~0u, ~0u, ~0u, ~0u};
        unsigned po = (unsigned)WS_O + (b * 16 + j * 4) * 1024 + lane * 16;
        __builtin_amdgcn_raw_buffer_store_b64(u32x2{~0u, ~0u}, rs, (unsigned)WS_ST + b * 2048 + j * 512 + lane * 8, 0, K1_PAUX);
        u32x4 v[4] = {};
        auto acc2 = [&](int s) {
            float lo[4], hi[4];
#pragma unroll
            for (int e = 0; e < 4; ++e) { lo[e] = bf_lo(v[s][e]); hi[e] = bf_hi(v[s][e]); }
            asm("v_add_f32 %0, %0, %4\n\tv_add_f32 %1, %1, %8\n\tv_fmac_f32 %2, %4, %4\n\tv_fmac_f32 %3, %8, %8\n\t"
                "v_add_f32 %0, %0, %5\n\tv_add_f32 %1, %1, %9\n\tv_fmac_f32 %2, %5, %5\n\tv_fmac_f32 %3, %9, %9\n\t"
                "v_add_f32 %0, %0, %6\n\tv_add_f32 %1, %1, %10\n\tv_fmac_f32 %2, %6, %6\n\tv_fmac_f32 %3, %10, %10\n\t"
                "v_add_f32 %0, %0, %7\n\tv_add_f32 %1, %1, %11\n\tv_fmac_f32 %2, %7, %7\n\tv_fmac_f32 %3, %11, %11"
                : "+v"(s0), "+v"(s1), "+v"(q0), "+v"(q1) : "v"(lo[0]), "v"(lo[1]), "v"(lo[2]), "v"(lo[3]), "v"(hi[0]), "v"(hi[1]), "v"(hi[2]), "v"(hi[3]));
        };
        int slot = 0;
#pragma nounroll
        for (int c = 0; c < 8; ++c) {
            WAITL_BAR();
            const unsigned sb = lds0 + slot * K1_SLOT + (32 * j + r) * 128;
            if (c & 1) { __builtin_amdgcn_raw_buffer_store_b128(sv, rs, po, 0, K1_PAUX); po += 1024; }
            v[0] = lds_ld128(sb + ((h ^ ((r >> 1) & 7)) * 16)); v[1] = lds_ld128(sb + (((2 + h) ^ ((r >> 1) & 7)) * 16));
            __builtin_amdgcn_sched_barrier(0);
            acc2(2); acc2(3);
            __builtin_amdgcn_sched_barrier(0);
            v[2] = lds_ld128(sb + (((4 + h) ^ ((r >> 1) & 7)) * 16)); v[3] = lds_ld128(sb + (((6 + h) ^ ((r >> 1) & 7)) * 16));
            __builtin_amdgcn_sched_barrier(0);
            acc2(0); acc2(1);
            __builtin_amdgcn_sched_barrier(0);
            slot = (slot == K1_NSLOT - 1) ? 0 : slot + 1;
        }
        acc2(2); acc2(3);
        const float mu = sum_halves(s0 + s1) * (1.f / DM);
        const float rstd = __builtin_amdgcn_rsqf(fmaxf(sum_halves(q0 + q1) * (1.f / DM) - mu * mu, 0.f) + LN_EPS);
        *(LAS float*)(lds0 + K1_ST + j * 256 + lane * 4) = h ? -mu * rstd : rstd;
        WAITL_BAR();
    }
}

constexpr int L_K = 0, L_V = 65536, L_ONE = 131072, L_R = L_ONE + 4096, L_CS = L_ONE, L_ST = L_CS + 1024, L_FLAG = L_ST + 1024, L_SINK = L_R + 24576, L_END = L_R + 28672;
#define WB_AUX 16
#define X_POISON 1
#define X_RETILE 1
#define X_WB 1
#define X_CNT 1
#define X_WARM 1
#define X_NOWD 0
#define X_NOEPI 0
#define X_FABAR 0
#define PROBE_SLEEP 2
constexpr unsigned SENT = 0xFFFFFFFFu;

typedef __amdgpu_buffer_rsrc_t rsrc_t;
__device__ __forceinline__ u32x4 ld_sc1(rsrc_t rs, unsigned voff, unsigned soff) { return __builtin_amdgcn_raw_buffer_load_b128(rs, voff, soff, 16); }
__device__ __forceinline__ void st_sc1(rsrc_t rs, unsigned voff, unsigned soff, u32x4 v) {
    __builtin_amdgcn_raw_buffer_store_b128(v, rs, voff, soff, 16);
    asm volatile("s_nop 1" :: "v"(v) : "memory");
}
__device__ __forceinline__ unsigned clean(unsigned v) { return min(v, 0xFFFFFFFEu); }
__device__ __forceinline__ unsigned umax4(unsigned m, u32x4 v) { return max(max(m, max(v[0], v[1])), max(v[2], v[3])); }
__device__ __forceinline__ void glds16_sc1(const void* base, unsigned voff, unsigned lds_dst) {
    unsigned keep;
    asm volatile("s_mov_b32 %0, m0\n\ts_mov_b32 m0, %3\n\ts_nop 0\n\tglobal_load_lds_dwordx4 %1, %2 sc1\n\ts_mov_b32 m0, %0"
                 : "=&s"(keep) : "v"(voff), "s"(base), "s"(rfl(lds_dst)) : "memory");
}
__device__ __forceinline__ float max16(const f32x16& s) {
    float m = fmaxf(s[0], s[1]);
#pragma unroll
    for (int i = 2; i < 16; ++i) m = fmaxf(m, s[i]);
    return m;
}
__device__ __forceinline__ f32x16 splat16(float v) { f32x16 r;
#pragma unroll
    for (int i = 0; i < 16; ++i) r[i] = v;
    return r; }
__device__ __forceinline__ unsigned piece(int idx) { return (unsigned)((idx >> 4) * 32768 + (idx & 15) * 1024); }

#define ATT_STAGE_A \
    "s_mov_b32 s88, m0\n\ts_bitcmp1_b32 %[flg], 2\n\ts_cselect_b64 vcc, -1, 0\n\ts_mov_b32 s90, 0xe000\n\ts_mov_b32 s91, 0\n\ts_mov_b64 s[86:87], %[kvs]\n\ts_mov_b32 s89, %[kvd]\n\t" \
    "v_mov_b32 v93, %[ka]\n\tv_mov_b32 v94, %[va]\n\tv_mov_b32 v95, %[vs]\n\tds_read_b128 v[68:71], v93\n\tds_read_b128 v[72:75], v93 offset:1024\n\tds_read_b128 v[76:79], v94\n\t" \
    "ds_read_b128 v[80:83], v94 offset:512\n\tv_mov_b32 v16, %[negm]\n\tv_mov_b32 v17, %[negm]\n\tv_mov_b32 v18, %[negm]\n\tv_mov_b32 v19, %[negm]\n\tv_mov_b32 v20, %[negm]\n\t" \
    "v_mov_b32 v21, %[negm]\n\tv_mov_b32 v22, %[negm]\n\tv_mov_b32 v23, %[negm]\n\tv_mov_b32 v24, %[negm]\n\tv_mov_b32 v25, %[negm]\n\tv_mov_b32 v26, %[negm]\n\t" \
    "v_mov_b32 v27, %[negm]\n\tv_mov_b32 v28, %[negm]\n\tv_mov_b32 v29, %[negm]\n\tv_mov_b32 v30, %[negm]\n\tv_mov_b32 v31, %[negm]\n\tv_mov_b32 v92, 0xdddddddd\n\t" \
    "s_waitcnt lgkmcnt(3)\n\tv_mfma_f32_32x32x16_bf16 v[32:47], v[68:71], v[64:67], v[16:31]\n\tv_mov_b64 v[0:1], 0\n\tv_mov_b64 v[2:3], 0\n\tv_mov_b64 v[4:5], 0\n\t" \
    "v_mov_b64 v[6:7], 0\n\tv_mov_b64 v[8:9], 0\n\tv_mov_b64 v[10:11], 0\n\tv_mov_b64 v[12:13], 0\n\tv_mov_b64 v[14:15], 0\n\tv_mov_b64 v[56:57], 0\n\tv_mov_b64 v[58:59], 0\n\t" \
    "v_mov_b64 v[60:61], 0\n\tv_mov_b64 v[62:63], 0\n\tv_mov_b64 v[88:89], 0\n\tv_mov_b64 v[90:91], 0\n\ts_waitcnt lgkmcnt(0)\n\tv_exp_f32 v32, v32\n\tv_exp_f32 v33, v33\n\t" \
    "v_smfmac_f32_32x32x32_bf16 v[0:15], v[88:91], v[56:63], v92\n\ts_mov_b32 m0, s89\n\ts_add_u32 s89, s89, 0x1000\n\tglobal_load_lds_dwordx4 %[l16], s[86:87]\n\t" \
    "global_load_lds_dwordx4 %[l16], s[86:87] offset:2048\n\ts_add_u32 s86, s86, 0x1000\n\ts_addc_u32 s87, s87, 0\n\tds_read_b128 v[68:71], v93 offset:2048\n\t" \
    "ds_read_b128 v[84:87], v94 offset:1024\n\tds_read_b128 v[88:91], v94 offset:1536\n\tv_exp_f32 v34, v34\n\tv_exp_f32 v35, v35\n\tv_exp_f32 v36, v36\n\ts_waitcnt lgkmcnt(3)\n\t" \
    "v_mfma_f32_32x32x16_bf16 v[48:63], v[72:75], v[64:67], v[16:31]\n\tv_exp_f32 v37, v37\n\tv_exp_f32 v38, v38\n\tv_exp_f32 v39, v39\n\tv_exp_f32 v40, v40\n\tv_exp_f32 v41, v41\n\t" \
    "v_exp_f32 v42, v42\n\tv_smfmac_f32_32x32x32_bf16 v[0:15], v[76:79], v[32:39], v92\n\tv_exp_f32 v43, v43\n\tv_exp_f32 v44, v44\n\tv_exp_f32 v45, v45\n\tv_exp_f32 v46, v46\n\t" \
    "v_exp_f32 v47, v47\n\tv_exp_f32 v48, v48\n\tv_exp_f32 v49, v49\n\tv_smfmac_f32_32x32x32_bf16 v[0:15], v[80:83], v[40:47], v92\n\ts_mov_b32 m0, s89\n\ts_add_u32 s89, s89, 0x1000\n\t" \
    "global_load_lds_dwordx4 %[l16], s[86:87]\n\tglobal_load_lds_dwordx4 %[l16], s[86:87] offset:2048\n\ts_add_u32 s86, s86, 0x1000\n\ts_addc_u32 s87, s87, 0\n\t" \
    "ds_read_b128 v[72:75], v93 offset:3072\n\tds_read_b128 v[76:79], v94 offset:2048\n\tds_read_b128 v[80:83], v94 offset:2560\n\tv_exp_f32 v50, v50\n\tv_exp_f32 v51, v51\n\t" \
    "v_exp_f32 v52, v52\n\ts_waitcnt lgkmcnt(3)\n\tv_mfma_f32_32x32x16_bf16 v[32:47], v[68:71], v[64:67], v[16:31]\n\tv_exp_f32 v53, v53\n\tv_exp_f32 v54, v54\n\tv_exp_f32 v55, v55\n\t" \
    "v_exp_f32 v56, v56\n\tv_exp_f32 v57, v57\n\tv_exp_f32 v58, v58\n\tv_smfmac_f32_32x32x32_bf16 v[0:15], v[84:87], v[48:55], v92\n\tv_exp_f32 v59, v59\n\tv_exp_f32 v60, v60\n\t" \
    "v_exp_f32 v61, v61\n\tv_exp_f32 v62, v62\n\tv_exp_f32 v63, v63\n\tv_exp_f32 v32, v32\n\tv_exp_f32 v33, v33\n\tv_smfmac_f32_32x32x32_bf16 v[0:15], v[88:91], v[56:63], v92\n\t" \
    "s_mov_b32 m0, s89\n\ts_add_u32 s89, s89, 0x1000\n\tglobal_load_lds_dwordx4 %[l16], s[86:87]\n\tglobal_load_lds_dwordx4 %[l16], s[86:87] offset:2048\n\t" \
    "s_add_u32 s86, s86, 0x1000\n\ts_addc_u32 s87, s87, 0\n\tds_read_b128 v[68:71], v93 offset:4096\n\tds_read_b128 v[84:87], v94 offset:3072\n\t" \
    "ds_read_b128 v[88:91], v94 offset:3584\n\tv_exp_f32 v34, v34\n\tv_exp_f32 v35, v35\n\tv_exp_f32 v36, v36\n\ts_waitcnt lgkmcnt(3)\n\t" \
    "v_mfma_f32_32x32x16_bf16 v[48:63], v[72:75], v[64:67], v[16:31]\n\tv_exp_f32 v37, v37\n\tv_exp_f32 v38, v38\n\tv_exp_f32 v39, v39\n\tv_exp_f32 v40, v40\n\tv_exp_f32 v41, v41\n\t" \
    "v_exp_f32 v42, v42\n\tv_smfmac_f32_32x32x32_bf16 v[0:15], v[76:79], v[32:39], v92\n\tv_exp_f32 v43, v43\n\tv_exp_f32 v44, v44\n\tv_exp_f32 v45, v45\n\tv_exp_f32 v46, v46\n\t" \
    "v_exp_f32 v47, v47\n\tv_exp_f32 v48, v48\n\tv_exp_f32 v49, v49\n\tv_smfmac_f32_32x32x32_bf16 v[0:15], v[80:83], v[40:47], v92\n\ts_mov_b32 m0, s89\n\ts_add_u32 s89, s89, 0x1000\n\t" \
    "global_load_lds_dwordx4 %[l16], s[86:87]\n\tglobal_load_lds_dwordx4 %[l16], s[86:87] offset:2048\n\ts_add_u32 s86, s86, 0x1000\n\ts_addc_u32 s87, s87, 0\n\t" \
    "v_add_u32 v94, v94, v95\n\tds_read_b128 v[72:75], v93 offset:5120\n\tds_read_b128 v[76:79], v94 offset:0\n\tds_read_b128 v[80:83], v94 offset:512\n\tv_exp_f32 v50, v50\n\t" \
    "v_exp_f32 v51, v51\n\tv_exp_f32 v52, v52\n\ts_waitcnt lgkmcnt(3)\n\tv_mfma_f32_32x32x16_bf16 v[32:47], v[68:71], v[64:67], v[16:31]\n\tv_exp_f32 v53, v53\n\tv_exp_f32 v54, v54\n\t" \
    "v_exp_f32 v55, v55\n\tv_exp_f32 v56, v56\n\tv_exp_f32 v57, v57\n\tv_exp_f32 v58, v58\n\tv_smfmac_f32_32x32x32_bf16 v[0:15], v[84:87], v[48:55], v92\n\tv_exp_f32 v59, v59\n\t" \
    "v_exp_f32 v60, v60\n\tv_exp_f32 v61, v61\n\tv_exp_f32 v62, v62\n\tv_exp_f32 v63, v63\n\tv_add_u32 v93, 0x1000, v93\n\ts_waitcnt vmcnt(9)\n\ts_barrier\n\ts_setprio 3\n\t" \
    "v_exp_f32 v32, v32\n\tv_exp_f32 v33, v33\n\tv_smfmac_f32_32x32x32_bf16 v[0:15], v[88:91], v[56:63], v92\n\ts_cbranch_vccz 1f\n\ts_add_u32 s89, %[wld], 0\n\ts_mov_b32 m0, s89\n\t" \
    "s_nop 0\n\tglobal_load_lds_dwordx4 %[wga], off\n\tv_lshl_add_u64 %[wga], s[90:91], 0, %[wga]\n\t1:\n\tds_read_b128 v[68:71], v93 offset:2048\n\t" \
    "ds_read_b128 v[84:87], v94 offset:1024\n\tds_read_b128 v[88:91], v94 offset:1536\n\tv_exp_f32 v34, v34\n\tv_exp_f32 v35, v35\n\tv_exp_f32 v36, v36\n\ts_waitcnt lgkmcnt(3)\n\t" \
    "v_mfma_f32_32x32x16_bf16 v[48:63], v[72:75], v[64:67], v[16:31]\n\tv_exp_f32 v37, v37\n\tv_exp_f32 v38, v38\n\tv_exp_f32 v39, v39\n\tv_exp_f32 v40, v40\n\tv_exp_f32 v41, v41\n\t" \
    "v_exp_f32 v42, v42\n\tv_smfmac_f32_32x32x32_bf16 v[0:15], v[76:79], v[32:39], v92\n\tv_exp_f32 v43, v43\n\tv_exp_f32 v44, v44\n\tv_exp_f32 v45, v45\n\tv_exp_f32 v46, v46\n\t" \
    "v_exp_f32 v47, v47\n\tv_exp_f32 v48, v48\n\tv_exp_f32 v49, v49\n\tv_smfmac_f32_32x32x32_bf16 v[0:15], v[80:83], v[40:47], v92\n\ts_cbranch_vccz 2f\n\t" \
    "s_add_u32 s89, %[wld], 1024\n\ts_mov_b32 m0, s89\n\ts_nop 0\n\tglobal_load_lds_dwordx4 %[wga], off\n\tv_lshl_add_u64 %[wga], s[90:91], 0, %[wga]\n\t2:\n\t" \
    "ds_read_b128 v[72:75], v93 offset:3072\n\tds_read_b128 v[76:79], v94 offset:2048\n\tds_read_b128 v[80:83], v94 offset:2560\n\tv_exp_f32 v50, v50\n\tv_exp_f32 v51, v51\n\t" \
    "v_exp_f32 v52, v52\n\ts_waitcnt lgkmcnt(3)\n\tv_mfma_f32_32x32x16_bf16 v[32:47], v[68:71], v[64:67], v[16:31]\n\tv_exp_f32 v53, v53\n\tv_exp_f32 v54, v54\n\tv_exp_f32 v55, v55\n\t" \
    "v_exp_f32 v56, v56\n\tv_exp_f32 v57, v57\n\tv_exp_f32 v58, v58\n\tv_smfmac_f32_32x32x32_bf16 v[0:15], v[84:87], v[48:55], v92\n\tv_exp_f32 v59, v59\n\tv_exp_f32 v60, v60\n\t" \
    "v_exp_f32 v61, v61\n\tv_exp_f32 v62, v62\n\tv_exp_f32 v63, v63\n\tv_exp_f32 v32, v32\n\tv_exp_f32 v33, v33\n\tv_smfmac_f32_32x32x32_bf16 v[0:15], v[88:91], v[56:63], v92\n\t" \
    "s_cbranch_vccz 3f\n\ts_add_u32 s89, %[wld], 2048\n\ts_mov_b32 m0, s89\n\ts_nop 0\n\tglobal_load_lds_dwordx4 %[wga], off\n\tv_lshl_add_u64 %[wga], s[90:91], 0, %[wga]\n\t3:\n\t" \
    "ds_read_b128 v[68:71], v93 offset:4096\n\tds_read_b128 v[84:87], v94 offset:3072\n\tds_read_b128 v[88:91], v94 offset:3584\n\tv_exp_f32 v34, v34\n\tv_exp_f32 v35, v35\n\t" \
    "v_exp_f32 v36, v36\n\ts_waitcnt lgkmcnt(3)\n\tv_mfma_f32_32x32x16_bf16 v[48:63], v[72:75], v[64:67], v[16:31]\n\tv_exp_f32 v37, v37\n\tv_exp_f32 v38, v38\n\tv_exp_f32 v39, v39\n\t" \
    "v_exp_f32 v40, v40\n\tv_exp_f32 v41, v41\n\tv_exp_f32 v42, v42\n\tv_smfmac_f32_32x32x32_bf16 v[0:15], v[76:79], v[32:39], v92\n\tv_exp_f32 v43, v43\n\tv_exp_f32 v44, v44\n\t" \
    "v_exp_f32 v45, v45\n\tv_exp_f32 v46, v46\n\tv_exp_f32 v47, v47\n\tv_exp_f32 v48, v48\n\tv_exp_f32 v49, v49\n\tv_smfmac_f32_32x32x32_bf16 v[0:15], v[80:83], v[40:47], v92\n\t" \
    "s_cbranch_vccz 4f\n\ts_add_u32 s89, %[wld], 3072\n\ts_mov_b32 m0, s89\n\ts_nop 0\n\tglobal_load_lds_dwordx4 %[wga], off\n\tv_lshl_add_u64 %[wga], s[90:91], 0, %[wga]\n\t" \
    "s_waitcnt vmcnt(3)\n\tds_read_b64_tr_b16 v[96:97], %[wta] offset:0\n\tds_read_b64_tr_b16 v[98:99], %[wta] offset:512\n\t4:\n\tv_add_u32 v94, v94, v95\n\t" \
    "ds_read_b128 v[72:75], v93 offset:5120\n\tds_read_b128 v[76:79], v94 offset:0\n\tds_read_b128 v[80:83], v94 offset:512\n\tv_exp_f32 v50, v50\n\tv_exp_f32 v51, v51\n\t" \
    "v_exp_f32 v52, v52\n\ts_waitcnt lgkmcnt(3)\n\tv_mfma_f32_32x32x16_bf16 v[32:47], v[68:71], v[64:67], v[16:31]\n\tv_exp_f32 v53, v53\n\tv_exp_f32 v54, v54\n\tv_exp_f32 v55, v55\n\t" \
    "v_exp_f32 v56, v56\n\tv_exp_f32 v57, v57\n\tv_exp_f32 v58, v58\n\tv_smfmac_f32_32x32x32_bf16 v[0:15], v[84:87], v[48:55], v92\n\tv_exp_f32 v59, v59\n\tv_exp_f32 v60, v60\n\t" \
    "v_exp_f32 v61, v61\n\tv_exp_f32 v62, v62\n\tv_exp_f32 v63, v63\n\tv_add_u32 v93, 0x1000, v93\n\ts_setprio 2\n\tv_exp_f32 v32, v32\n\tv_exp_f32 v33, v33\n\t" \
    "v_smfmac_f32_32x32x32_bf16 v[0:15], v[88:91], v[56:63], v92\n\ts_cbranch_vccz 5f\n\ts_add_u32 s89, %[wld], 0\n\ts_mov_b32 m0, s89\n\ts_nop 0\n\t" \
    "global_load_lds_dwordx4 %[wga], off\n\tv_lshl_add_u64 %[wga], s[90:91], 0, %[wga]\n\ts_waitcnt vmcnt(3)\n\tds_read_b64_tr_b16 v[100:101], %[wta] offset:1024\n\t" \
    "ds_read_b64_tr_b16 v[102:103], %[wta] offset:1536\n\t5:\n\tds_read_b128 v[68:71], v93 offset:2048\n\tds_read_b128 v[84:87], v94 offset:1024\n\t" \
    "ds_read_b128 v[88:91], v94 offset:1536\n\tv_exp_f32 v34, v34\n\tv_exp_f32 v35, v35\n\tv_exp_f32 v36, v36\n\ts_waitcnt lgkmcnt(3)\n\t" \
    "v_mfma_f32_32x32x16_bf16 v[48:63], v[72:75], v[64:67], v[16:31]\n\tv_exp_f32 v37, v37\n\tv_exp_f32 v38, v38\n\tv_exp_f32 v39, v39\n\tv_exp_f32 v40, v40\n\tv_exp_f32 v41, v41\n\t" \
    "v_exp_f32 v42, v42\n\tv_smfmac_f32_32x32x32_bf16 v[0:15], v[76:79], v[32:39], v92\n\tv_exp_f32 v43, v43\n\tv_exp_f32 v44, v44\n\tv_exp_f32 v45, v45\n\tv_exp_f32 v46, v46\n\t" \
    "v_exp_f32 v47, v47\n\tv_exp_f32 v48, v48\n\tv_exp_f32 v49, v49\n\tv_smfmac_f32_32x32x32_bf16 v[0:15], v[80:83], v[40:47], v92\n\ts_cbranch_vccz 6f\n\t" \
    "s_add_u32 s89, %[wld], 1024\n\ts_mov_b32 m0, s89\n\ts_nop 0\n\tglobal_load_lds_dwordx4 %[wga], off\n\tv_lshl_add_u64 %[wga], s[90:91], 0, %[wga]\n\ts_waitcnt vmcnt(3)\n\t" \
    "ds_read_b64_tr_b16 v[104:105], %[wta] offset:2048\n\tds_read_b64_tr_b16 v[106:107], %[wta] offset:2560\n\t6:\n\tds_read_b128 v[72:75], v93 offset:3072\n\t" \
    "ds_read_b128 v[76:79], v94 offset:2048\n\tds_read_b128 v[80:83], v94 offset:2560\n\tv_exp_f32 v50, v50\n\tv_exp_f32 v51, v51\n\tv_exp_f32 v52, v52\n\ts_waitcnt lgkmcnt(3)\n\t" \
    "v_mfma_f32_32x32x16_bf16 v[32:47], v[68:71], v[64:67], v[16:31]\n\tv_exp_f32 v53, v53\n\tv_exp_f32 v54, v54\n\tv_exp_f32 v55, v55\n\tv_exp_f32 v56, v56\n\tv_exp_f32 v57, v57\n\t" \
    "v_exp_f32 v58, v58\n\tv_smfmac_f32_32x32x32_bf16 v[0:15], v[84:87], v[48:55], v92\n\tv_exp_f32 v59, v59\n\tv_exp_f32 v60, v60\n\tv_exp_f32 v61, v61\n\tv_exp_f32 v62, v62\n\t" \
    "v_exp_f32 v63, v63\n\tv_exp_f32 v32, v32\n\tv_exp_f32 v33, v33\n\tv_smfmac_f32_32x32x32_bf16 v[0:15], v[88:91], v[56:63], v92\n\ts_cbranch_vccz 7f\n\t" \
    "s_add_u32 s89, %[wld], 2048\n\ts_mov_b32 m0, s89\n\ts_nop 0\n\tglobal_load_lds_dwordx4 %[wga], off\n\tv_lshl_add_u64 %[wga], s[90:91], 0, %[wga]\n\ts_waitcnt vmcnt(3)\n\t" \
    "ds_read_b64_tr_b16 v[108:109], %[wta] offset:3072\n\tds_read_b64_tr_b16 v[110:111], %[wta] offset:3584\n\t7:\n\tds_read_b128 v[68:71], v93 offset:4096\n\t" \
    "ds_read_b128 v[84:87], v94 offset:3072\n\tds_read_b128 v[88:91], v94 offset:3584\n\tv_exp_f32 v34, v34\n\tv_exp_f32 v35, v35\n\tv_exp_f32 v36, v36\n\ts_waitcnt lgkmcnt(3)\n\t" \
    "v_mfma_f32_32x32x16_bf16 v[48:63], v[72:75], v[64:67], v[16:31]\n\tv_exp_f32 v37, v37\n\tv_exp_f32 v38, v38\n\tv_exp_f32 v39, v39\n\tv_exp_f32 v40, v40\n\tv_exp_f32 v41, v41\n\t" \
    "v_exp_f32 v42, v42\n\tv_smfmac_f32_32x32x32_bf16 v[0:15], v[76:79], v[32:39], v92\n\tv_exp_f32 v43, v43\n\tv_exp_f32 v44, v44\n\tv_exp_f32 v45, v45\n\tv_exp_f32 v46, v46\n\t" \
    "v_exp_f32 v47, v47\n\tv_exp_f32 v48, v48\n\tv_exp_f32 v49, v49\n\tv_smfmac_f32_32x32x32_bf16 v[0:15], v[80:83], v[40:47], v92\n\ts_cbranch_vccz 8f\n\t" \
    "s_add_u32 s89, %[wld], 3072\n\ts_mov_b32 m0, s89\n\ts_nop 0\n\tglobal_load_lds_dwordx4 %[wga], off\n\tv_lshl_add_u64 %[wga], s[90:91], 0, %[wga]\n\ts_waitcnt vmcnt(3)\n\t" \
    "ds_read_b64_tr_b16 v[112:113], %[wta] offset:0\n\tds_read_b64_tr_b16 v[114:115], %[wta] offset:512\n\t8:\n\tv_add_u32 v94, v94, v95\n\tds_read_b128 v[72:75], v93 offset:5120\n\t" \
    "ds_read_b128 v[76:79], v94 offset:0\n\tds_read_b128 v[80:83], v94 offset:512\n\tv_exp_f32 v50, v50\n\tv_exp_f32 v51, v51\n\tv_exp_f32 v52, v52\n\ts_waitcnt lgkmcnt(3)\n\t" \
    "v_mfma_f32_32x32x16_bf16 v[32:47], v[68:71], v[64:67], v[16:31]\n\tv_exp_f32 v53, v53\n\tv_exp_f32 v54, v54\n\tv_exp_f32 v55, v55\n\tv_exp_f32 v56, v56\n\tv_exp_f32 v57, v57\n\t" \
    "v_exp_f32 v58, v58\n\tv_smfmac_f32_32x32x32_bf16 v[0:15], v[84:87], v[48:55], v92\n\tv_exp_f32 v59, v59\n\tv_exp_f32 v60, v60\n\tv_exp_f32 v61, v61\n\tv_exp_f32 v62, v62\n\t" \
    "v_exp_f32 v63, v63\n\tv_add_u32 v93, 0x1000, v93\n\ts_cbranch_vccnz 20f\n\ts_waitcnt vmcnt(0)\n\t20:\n\ts_barrier\n\ts_setprio 3\n\tv_exp_f32 v32, v32\n\tv_exp_f32 v33, v33\n\t" \
    "v_smfmac_f32_32x32x32_bf16 v[0:15], v[88:91], v[56:63], v92\n\ts_cbranch_vccz 9f\n\ts_add_u32 s89, %[wld], 0\n\ts_mov_b32 m0, s89\n\ts_nop 0\n\t" \
    "global_load_lds_dwordx4 %[wga], off\n\tv_lshl_add_u64 %[wga], s[90:91], 0, %[wga]\n\ts_waitcnt vmcnt(3)\n\tds_read_b64_tr_b16 v[116:117], %[wta] offset:1024\n\t" \
    "ds_read_b64_tr_b16 v[118:119], %[wta] offset:1536\n\t9:\n\tds_read_b128 v[68:71], v93 offset:2048\n\tds_read_b128 v[84:87], v94 offset:1024\n\t" \
    "ds_read_b128 v[88:91], v94 offset:1536\n\tv_exp_f32 v34, v34\n\tv_exp_f32 v35, v35\n\tv_exp_f32 v36, v36\n\ts_waitcnt lgkmcnt(3)\n\t" \
    "v_mfma_f32_32x32x16_bf16 v[48:63], v[72:75], v[64:67], v[16:31]\n\tv_exp_f32 v37, v37\n\tv_exp_f32 v38, v38\n\tv_exp_f32 v39, v39\n\tv_exp_f32 v40, v40\n\tv_exp_f32 v41, v41\n\t" \
    "v_exp_f32 v42, v42\n\tv_smfmac_f32_32x32x32_bf16 v[0:15], v[76:79], v[32:39], v92\n\tv_exp_f32 v43, v43\n\tv_exp_f32 v44, v44\n\tv_exp_f32 v45, v45\n\tv_exp_f32 v46, v46\n\t" \
    "v_exp_f32 v47, v47\n\tv_exp_f32 v48, v48\n\tv_exp_f32 v49, v49\n\tv_smfmac_f32_32x32x32_bf16 v[0:15], v[80:83], v[40:47], v92\n\ts_cbranch_vccz 10f\n\t" \
    "s_add_u32 s89, %[wld], 1024\n\ts_mov_b32 m0, s89\n\ts_nop 0\n\tglobal_load_lds_dwordx4 %[wga], off\n\tv_lshl_add_u64 %[wga], s[90:91], 0, %[wga]\n\ts_waitcnt vmcnt(3)\n\t" \
    "ds_read_b64_tr_b16 v[120:121], %[wta] offset:2048\n\tds_read_b64_tr_b16 v[122:123], %[wta] offset:2560\n\t10:\n\tds_read_b128 v[72:75], v93 offset:3072\n\t" \
    "ds_read_b128 v[76:79], v94 offset:2048\n\tds_read_b128 v[80:83], v94 offset:2560\n\tv_exp_f32 v50, v50\n\tv_exp_f32 v51, v51\n\tv_exp_f32 v52, v52\n\ts_waitcnt lgkmcnt(3)\n\t" \
    "v_mfma_f32_32x32x16_bf16 v[32:47], v[68:71], v[64:67], v[16:31]\n\tv_exp_f32 v53, v53\n\tv_exp_f32 v54, v54\n\tv_exp_f32 v55, v55\n\tv_exp_f32 v56, v56\n\tv_exp_f32 v57, v57\n\t" \
    "v_exp_f32 v58, v58\n\tv_smfmac_f32_32x32x32_bf16 v[0:15], v[84:87], v[48:55], v92\n\tv_exp_f32 v59, v59\n\tv_exp_f32 v60, v60\n\tv_exp_f32 v61, v61\n\tv_exp_f32 v62, v62\n\t" \
    "v_exp_f32 v63, v63\n\tv_exp_f32 v32, v32\n\tv_exp_f32 v33, v33\n\tv_smfmac_f32_32x32x32_bf16 v[0:15], v[88:91], v[56:63], v92\n\ts_cbranch_vccz 11f\n\t" \
    "s_add_u32 s89, %[wld], 2048\n\ts_mov_b32 m0, s89\n\ts_nop 0\n\tglobal_load_lds_dwordx4 %[wga], off\n\tv_lshl_add_u64 %[wga], s[90:91], 0, %[wga]\n\ts_waitcnt vmcnt(3)\n\t" \
    "ds_read_b64_tr_b16 v[124:125], %[wta] offset:3072\n\tds_read_b64_tr_b16 v[126:127], %[wta] offset:3584\n\t11:\n\tds_read_b128 v[68:71], v93 offset:4096\n\t" \
    "ds_read_b128 v[84:87], v94 offset:3072\n\tds_read_b128 v[88:91], v94 offset:3584\n\tv_exp_f32 v34, v34\n\tv_exp_f32 v35, v35\n\tv_exp_f32 v36, v36\n\ts_waitcnt lgkmcnt(3)\n\t" \
    "v_mfma_f32_32x32x16_bf16 v[48:63], v[72:75], v[64:67], v[16:31]\n\tv_exp_f32 v37, v37\n\tv_exp_f32 v38, v38\n\tv_exp_f32 v39, v39\n\tv_exp_f32 v40, v40\n\tv_exp_f32 v41, v41\n\t" \
    "v_exp_f32 v42, v42\n\tv_smfmac_f32_32x32x32_bf16 v[0:15], v[76:79], v[32:39], v92\n\tv_exp_f32 v43, v43\n\tv_exp_f32 v44, v44\n\tv_exp_f32 v45, v45\n\tv_exp_f32 v46, v46\n\t" \
    "v_exp_f32 v47, v47\n\tv_exp_f32 v48, v48\n\tv_exp_f32 v49, v49\n\tv_smfmac_f32_32x32x32_bf16 v[0:15], v[80:83], v[40:47], v92\n\ts_cbranch_vccz 12f\n\t" \
    "s_add_u32 s89, %[wld], 3072\n\ts_mov_b32 m0, s89\n\ts_nop 0\n\tglobal_load_lds_dwordx4 %[wga], off\n\tv_lshl_add_u64 %[wga], s[90:91], 0, %[wga]\n\ts_waitcnt vmcnt(3)\n\t" \
    "ds_read_b64_tr_b16 v[128:129], %[wta] offset:0\n\tds_read_b64_tr_b16 v[130:131], %[wta] offset:512\n\t12:\n\tv_add_u32 v94, v94, v95\n\tds_read_b128 v[72:75], v93 offset:5120\n\t" \
    "ds_read_b128 v[76:79], v94 offset:0\n\tds_read_b128 v[80:83], v94 offset:512\n\tv_exp_f32 v50, v50\n\tv_exp_f32 v51, v51\n\tv_exp_f32 v52, v52\n\ts_waitcnt lgkmcnt(3)\n\t" \
    "v_mfma_f32_32x32x16_bf16 v[32:47], v[68:71], v[64:67], v[16:31]\n\tv_exp_f32 v53, v53\n\tv_exp_f32 v54, v54\n\tv_exp_f32 v55, v55\n\tv_exp_f32 v56, v56\n\tv_exp_f32 v57, v57\n\t" \
    "v_exp_f32 v58, v58\n\tv_smfmac_f32_32x32x32_bf16 v[0:15], v[84:87], v[48:55], v92\n\tv_exp_f32 v59, v59\n\tv_exp_f32 v60, v60\n\tv_exp_f32 v61, v61\n\tv_exp_f32 v62, v62\n\t" \
    "v_exp_f32 v63, v63\n\tv_add_u32 v93, 0x1000, v93\n\tv_exp_f32 v32, v32\n\tv_exp_f32 v33, v33\n\tv_smfmac_f32_32x32x32_bf16 v[0:15], v[88:91], v[56:63], v92\n\t" \
    "s_cbranch_vccz 13f\n\ts_add_u32 s89, %[wld], 0\n\ts_mov_b32 m0, s89\n\ts_nop 0\n\tglobal_load_lds_dwordx4 %[wga], off\n\tv_lshl_add_u64 %[wga], s[90:91], 0, %[wga]\n\t" \
    "s_waitcnt vmcnt(3)\n\tds_read_b64_tr_b16 v[132:133], %[wta] offset:1024\n\tds_read_b64_tr_b16 v[134:135], %[wta] offset:1536\n\t13:\n\tds_read_b128 v[68:71], v93 offset:2048\n\t" \
    "ds_read_b128 v[84:87], v94 offset:1024\n\tds_read_b128 v[88:91], v94 offset:1536\n\tv_exp_f32 v34, v34\n\tv_exp_f32 v35, v35\n\tv_exp_f32 v36, v36\n\ts_waitcnt lgkmcnt(3)\n\t" \
    "v_mfma_f32_32x32x16_bf16 v[48:63], v[72:75], v[64:67], v[16:31]\n\tv_exp_f32 v37, v37\n\tv_exp_f32 v38, v38\n\tv_exp_f32 v39, v39\n\tv_exp_f32 v40, v40\n\tv_exp_f32 v41, v41\n\t" \
    "v_exp_f32 v42, v42\n\tv_smfmac_f32_32x32x32_bf16 v[0:15], v[76:79], v[32:39], v92\n\tv_exp_f32 v43, v43\n\tv_exp_f32 v44, v44\n\tv_exp_f32 v45, v45\n\tv_exp_f32 v46, v46\n\t" \
    "v_exp_f32 v47, v47\n\tv_exp_f32 v48, v48\n\tv_exp_f32 v49, v49\n\tv_smfmac_f32_32x32x32_bf16 v[0:15], v[80:83], v[40:47], v92\n\ts_cbranch_vccz 14f\n\t" \
    "s_add_u32 s89, %[wld], 1024\n\ts_mov_b32 m0, s89\n\ts_nop 0\n\tglobal_load_lds_dwordx4 %[wga], off\n\tv_lshl_add_u64 %[wga], s[90:91], 0, %[wga]\n\ts_waitcnt vmcnt(3)\n\t" \
    "ds_read_b64_tr_b16 v[136:137], %[wta] offset:2048\n\tds_read_b64_tr_b16 v[138:139], %[wta] offset:2560\n\t14:\n\tds_read_b128 v[72:75], v93 offset:3072\n\t" \
    "ds_read_b128 v[76:79], v94 offset:2048\n\tds_read_b128 v[80:83], v94 offset:2560\n\tv_exp_f32 v50, v50\n\tv_exp_f32 v51, v51\n\tv_exp_f32 v52, v52\n\ts_waitcnt lgkmcnt(3)\n\t" \
    "v_mfma_f32_32x32x16_bf16 v[32:47], v[68:71], v[64:67], v[16:31]\n\tv_exp_f32 v53, v53\n\tv_exp_f32 v54, v54\n\tv_exp_f32 v55, v55\n\tv_exp_f32 v56, v56\n\tv_exp_f32 v57, v57\n\t" \
    "v_exp_f32 v58, v58\n\tv_smfmac_f32_32x32x32_bf16 v[0:15], v[84:87], v[48:55], v92\n\tv_exp_f32 v59, v59\n\tv_exp_f32 v60, v60\n\tv_exp_f32 v61, v61\n\tv_exp_f32 v62, v62\n\t" \
    "v_exp_f32 v63, v63\n\ts_setprio 2\n\tv_exp_f32 v32, v32\n\tv_exp_f32 v33, v33\n\tv_smfmac_f32_32x32x32_bf16 v[0:15], v[88:91], v[56:63], v92\n\ts_cbranch_vccz 15f\n\t" \
    "s_add_u32 s89, %[wld], 2048\n\ts_mov_b32 m0, s89\n\ts_nop 0\n\tglobal_load_lds_dwordx4 %[wga], off\n\tv_lshl_add_u64 %[wga], s[90:91], 0, %[wga]\n\ts_waitcnt vmcnt(3)\n\t" \
    "ds_read_b64_tr_b16 v[140:141], %[wta] offset:3072\n\tds_read_b64_tr_b16 v[142:143], %[wta] offset:3584\n\t15:\n\tds_read_b128 v[68:71], v93 offset:4096\n\t" \
    "ds_read_b128 v[84:87], v94 offset:3072\n\tds_read_b128 v[88:91], v94 offset:3584\n\tv_exp_f32 v34, v34\n\tv_exp_f32 v35, v35\n\tv_exp_f32 v36, v36\n\ts_waitcnt lgkmcnt(3)\n\t" \
    "v_mfma_f32_32x32x16_bf16 v[48:63], v[72:75], v[64:67], v[16:31]\n\tv_exp_f32 v37, v37\n\tv_exp_f32 v38, v38\n\tv_exp_f32 v39, v39\n\tv_exp_f32 v40, v40\n\tv_exp_f32 v41, v41\n\t" \
    "v_exp_f32 v42, v42\n\tv_smfmac_f32_32x32x32_bf16 v[0:15], v[76:79], v[32:39], v92\n\tv_exp_f32 v43, v43\n\tv_exp_f32 v44, v44\n\tv_exp_f32 v45, v45\n\tv_exp_f32 v46, v46\n\t" \
    "v_exp_f32 v47, v47\n\tv_exp_f32 v48, v48\n\tv_exp_f32 v49, v49\n\tv_smfmac_f32_32x32x32_bf16 v[0:15], v[80:83], v[40:47], v92\n\ts_cbranch_vccz 16f\n\t" \
    "s_add_u32 s89, %[wld], 3072\n\ts_mov_b32 m0, s89\n\ts_nop 0\n\tglobal_load_lds_dwordx4 %[wga], off\n\tv_lshl_add_u64 %[wga], s[90:91], 0, %[wga]\n\ts_waitcnt vmcnt(3)\n\t" \
    "ds_read_b64_tr_b16 v[144:145], %[wta] offset:0\n\tds_read_b64_tr_b16 v[146:147], %[wta] offset:512\n\t16:\n\tv_add_u32 v94, v94, v95\n\tds_read_b128 v[72:75], v93 offset:5120\n\t" \
    "ds_read_b128 v[76:79], v94 offset:0\n\tds_read_b128 v[80:83], v94 offset:512\n\tv_exp_f32 v50, v50\n\tv_exp_f32 v51, v51\n\tv_exp_f32 v52, v52\n\ts_waitcnt lgkmcnt(3)\n\t" \
    "v_mfma_f32_32x32x16_bf16 v[32:47], v[68:71], v[64:67], v[16:31]\n\tv_exp_f32 v53, v53\n\tv_exp_f32 v54, v54\n\tv_exp_f32 v55, v55\n\tv_exp_f32 v56, v56\n\tv_exp_f32 v57, v57\n\t" \
    "v_exp_f32 v58, v58\n\tv_smfmac_f32_32x32x32_bf16 v[0:15], v[84:87], v[48:55], v92\n\tv_exp_f32 v59, v59\n\tv_exp_f32 v60, v60\n\tv_exp_f32 v61, v61\n\tv_exp_f32 v62, v62\n\t" \
    "v_exp_f32 v63, v63\n\tv_add_u32 v93, 0x1000, v93\n\tv_exp_f32 v32, v32\n\tv_exp_f32 v33, v33\n\tv_smfmac_f32_32x32x32_bf16 v[0:15], v[88:91], v[56:63], v92\n\t" \
    "s_cbranch_vccz 17f\n\ts_waitcnt vmcnt(2)\n\tds_read_b64_tr_b16 v[148:149], %[wta] offset:1024\n\tds_read_b64_tr_b16 v[150:151], %[wta] offset:1536\n\t17:\n\t" \
    "ds_read_b128 v[68:71], v93 offset:2048\n\tds_read_b128 v[84:87], v94 offset:1024\n\tds_read_b128 v[88:91], v94 offset:1536\n\tv_exp_f32 v34, v34\n\tv_exp_f32 v35, v35\n\t" \
    "v_exp_f32 v36, v36\n\ts_waitcnt lgkmcnt(3)\n\tv_mfma_f32_32x32x16_bf16 v[48:63], v[72:75], v[64:67], v[16:31]\n\tv_exp_f32 v37, v37\n\tv_exp_f32 v38, v38\n\tv_exp_f32 v39, v39\n\t" \
    "v_exp_f32 v40, v40\n\tv_exp_f32 v41, v41\n\tv_exp_f32 v42, v42\n\tv_smfmac_f32_32x32x32_bf16 v[0:15], v[76:79], v[32:39], v92\n\tv_exp_f32 v43, v43\n\tv_exp_f32 v44, v44\n\t" \
    "v_exp_f32 v45, v45\n\tv_exp_f32 v46, v46\n\tv_exp_f32 v47, v47\n\tv_exp_f32 v48, v48\n\tv_exp_f32 v49, v49\n\tv_smfmac_f32_32x32x32_bf16 v[0:15], v[80:83], v[40:47], v92\n\t" \
    "s_cbranch_vccz 18f\n\ts_waitcnt vmcnt(1)\n\tds_read_b64_tr_b16 v[152:153], %[wta] offset:2048\n\tds_read_b64_tr_b16 v[154:155], %[wta] offset:2560\n\t18:\n\t" \
    "ds_read_b128 v[72:75], v93 offset:3072\n\tds_read_b128 v[76:79], v94 offset:2048\n\tds_read_b128 v[80:83], v94 offset:2560\n\tv_exp_f32 v50, v50\n\tv_exp_f32 v51, v51\n\t" \
    "v_exp_f32 v52, v52\n\ts_waitcnt lgkmcnt(3)\n\tv_mfma_f32_32x32x16_bf16 v[32:47], v[68:71], v[64:67], v[16:31]\n\tv_exp_f32 v53, v53\n\tv_exp_f32 v54, v54\n\tv_exp_f32 v55, v55\n\t" \
    "v_exp_f32 v56, v56\n\tv_exp_f32 v57, v57\n\tv_exp_f32 v58, v58\n\tv_smfmac_f32_32x32x32_bf16 v[0:15], v[84:87], v[48:55], v92\n\tv_exp_f32 v59, v59\n\tv_exp_f32 v60, v60\n\t" \
    "v_exp_f32 v61, v61\n\tv_exp_f32 v62, v62\n\tv_exp_f32 v63, v63\n\ts_setprio 1\n\tv_exp_f32 v32, v32\n\tv_exp_f32 v33, v33\n\t" \
    "v_smfmac_f32_32x32x32_bf16 v[0:15], v[88:91], v[56:63], v92\n\ts_cbranch_vccz 19f\n\ts_waitcnt vmcnt(0)\n\tds_read_b64_tr_b16 v[156:157], %[wta] offset:3072\n\t" \
    "ds_read_b64_tr_b16 v[158:159], %[wta] offset:3584\n\t19:\n\tds_read_b128 v[68:71], v93 offset:4096\n\tds_read_b128 v[84:87], v94 offset:3072\n\t" \
    "ds_read_b128 v[88:91], v94 offset:3584\n\tv_exp_f32 v34, v34\n\tv_exp_f32 v35, v35\n\tv_exp_f32 v36, v36\n\ts_waitcnt lgkmcnt(3)\n\t" \
    "v_mfma_f32_32x32x16_bf16 v[48:63], v[72:75], v[64:67], v[16:31]\n\tv_exp_f32 v37, v37\n\tv_exp_f32 v38, v38\n\tv_exp_f32 v39, v39\n\tv_exp_f32 v40, v40\n\tv_exp_f32 v41, v41\n\t" \
    "v_exp_f32 v42, v42\n\tv_smfmac_f32_32x32x32_bf16 v[0:15], v[76:79], v[32:39], v92\n\tv_exp_f32 v43, v43\n\tv_exp_f32 v44, v44\n\tv_exp_f32 v45, v45\n\tv_exp_f32 v46, v46\n\t" \
    "v_exp_f32 v47, v47\n\tv_exp_f32 v48, v48\n\tv_exp_f32 v49, v49\n\tv_smfmac_f32_32x32x32_bf16 v[0:15], v[80:83], v[40:47], v92\n\tv_add_u32 v94, v94, v95\n\t" \
    "ds_read_b128 v[72:75], v93 offset:5120\n\tds_read_b128 v[76:79], v94 offset:0\n\tds_read_b128 v[80:83], v94 offset:512\n\tv_exp_f32 v50, v50\n\tv_exp_f32 v51, v51\n\t" \
    "v_exp_f32 v52, v52\n\ts_waitcnt lgkmcnt(3)\n\tv_mfma_f32_32x32x16_bf16 v[32:47], v[68:71], v[64:67], v[16:31]\n\tv_exp_f32 v53, v53\n\tv_exp_f32 v54, v54\n\tv_exp_f32 v55, v55\n\t" \
    "v_exp_f32 v56, v56\n\tv_exp_f32 v57, v57\n\tv_exp_f32 v58, v58\n\tv_smfmac_f32_32x32x32_bf16 v[0:15], v[84:87], v[48:55], v92\n\tv_exp_f32 v59, v59\n\tv_exp_f32 v60, v60\n\t" \
    "v_exp_f32 v61, v61\n\tv_exp_f32 v62, v62\n\tv_exp_f32 v63, v63\n\tv_add_u32 v93, 0x1000, v93\n\ts_barrier\n\ts_setprio 3\n\tv_mov_b32 v160, -1\n\tv_mov_b32 v161, -1\n\t" \
    "v_mov_b32 v162, -1\n\tv_mov_b32 v163, -1\n\tv_exp_f32 v32, v32\n\tv_exp_f32 v33, v33\n\tv_smfmac_f32_32x32x32_bf16 v[0:15], v[88:91], v[56:63], v92\n\ts_cbranch_vccz 21f\n\t" \
    "s_add_u32 s89, %[pa], 0\n\tbuffer_store_dwordx4 v[160:163], %[l16], %[rs], s89 offen sc1\n\t21:\n\tds_read_b128 v[68:71], v93 offset:2048\n\t" \
    "ds_read_b128 v[84:87], v94 offset:1024\n\tds_read_b128 v[88:91], v94 offset:1536\n\tv_exp_f32 v34, v34\n\tv_exp_f32 v35, v35\n\tv_exp_f32 v36, v36\n\ts_waitcnt lgkmcnt(3)\n\t" \
    "v_mfma_f32_32x32x16_bf16 v[48:63], v[72:75], v[64:67], v[16:31]\n\tv_exp_f32 v37, v37\n\tv_exp_f32 v38, v38\n\tv_exp_f32 v39, v39\n\tv_exp_f32 v40, v40\n\tv_exp_f32 v41, v41\n\t" \
    "v_exp_f32 v42, v42\n\tv_smfmac_f32_32x32x32_bf16 v[0:15], v[76:79], v[32:39], v92\n\tv_exp_f32 v43, v43\n\tv_exp_f32 v44, v44\n\tv_exp_f32 v45, v45\n\tv_exp_f32 v46, v46\n\t" \
    "v_exp_f32 v47, v47\n\tv_exp_f32 v48, v48\n\tv_exp_f32 v49, v49\n\tv_smfmac_f32_32x32x32_bf16 v[0:15], v[80:83], v[40:47], v92\n\ts_cbranch_vccz 22f\n\t" \
    "s_add_u32 s89, %[pa], 114688\n\tbuffer_store_dwordx4 v[160:163], %[l16], %[rs], s89 offen sc1\n\t22:\n\tds_read_b128 v[72:75], v93 offset:3072\n\t" \
    "ds_read_b128 v[76:79], v94 offset:2048\n\tds_read_b128 v[80:83], v94 offset:2560\n\tv_exp_f32 v50, v50\n\tv_exp_f32 v51, v51\n\tv_exp_f32 v52, v52\n\ts_waitcnt lgkmcnt(3)\n\t" \
    "v_mfma_f32_32x32x16_bf16 v[32:47], v[68:71], v[64:67], v[16:31]\n\tv_exp_f32 v53, v53\n\tv_exp_f32 v54, v54\n\tv_exp_f32 v55, v55\n\tv_exp_f32 v56, v56\n\tv_exp_f32 v57, v57\n\t" \
    "v_exp_f32 v58, v58\n\tv_smfmac_f32_32x32x32_bf16 v[0:15], v[84:87], v[48:55], v92\n\tv_exp_f32 v59, v59\n\tv_exp_f32 v60, v60\n\tv_exp_f32 v61, v61\n\tv_exp_f32 v62, v62\n\t" \
    "v_exp_f32 v63, v63\n\tv_exp_f32 v32, v32\n\tv_exp_f32 v33, v33\n\tv_smfmac_f32_32x32x32_bf16 v[0:15], v[88:91], v[56:63], v92\n\ts_cbranch_vccz 23f\n\t" \
    "s_add_u32 s89, %[pa], 458752\n\tbuffer_store_dwordx4 v[160:163], %[l16], %[rs], s89 offen sc1\n\t23:\n\tds_read_b128 v[68:71], v93 offset:4096\n\t" \
    "ds_read_b128 v[84:87], v94 offset:3072\n\tds_read_b128 v[88:91], v94 offset:3584\n\tv_exp_f32 v34, v34\n\tv_exp_f32 v35, v35\n\tv_exp_f32 v36, v36\n\ts_waitcnt lgkmcnt(3)\n\t" \
    "v_mfma_f32_32x32x16_bf16 v[48:63], v[72:75], v[64:67], v[16:31]\n\tv_exp_f32 v37, v37\n\tv_exp_f32 v38, v38\n\tv_exp_f32 v39, v39\n\tv_exp_f32 v40, v40\n\tv_exp_f32 v41, v41\n\t" \
    "v_exp_f32 v42, v42\n\tv_smfmac_f32_32x32x32_bf16 v[0:15], v[76:79], v[32:39], v92\n\tv_exp_f32 v43, v43\n\tv_exp_f32 v44, v44\n\tv_exp_f32 v45, v45\n\tv_exp_f32 v46, v46\n\t" \
    "v_exp_f32 v47, v47\n\tv_exp_f32 v48, v48\n\tv_exp_f32 v49, v49\n\tv_smfmac_f32_32x32x32_bf16 v[0:15], v[80:83], v[40:47], v92\n\ts_cbranch_vccz 24f\n\t" \
    "s_add_u32 s89, %[pa], 573440\n\tbuffer_store_dwordx4 v[160:163], %[l16], %[rs], s89 offen sc1\n\t24:\n\tv_add_u32 v94, v94, v95\n\tds_read_b128 v[72:75], v93 offset:5120\n\t" \
    "ds_read_b128 v[76:79], v94 offset:0\n\tds_read_b128 v[80:83], v94 offset:512\n\tv_exp_f32 v50, v50\n\tv_exp_f32 v51, v51\n\tv_exp_f32 v52, v52\n\ts_waitcnt lgkmcnt(3)\n\t" \
    "v_mfma_f32_32x32x16_bf16 v[32:47], v[68:71], v[64:67], v[16:31]\n\tv_exp_f32 v53, v53\n\tv_exp_f32 v54, v54\n\tv_exp_f32 v55, v55\n\tv_exp_f32 v56, v56\n\tv_exp_f32 v57, v57\n\t" \
    "v_exp_f32 v58, v58\n\tv_smfmac_f32_32x32x32_bf16 v[0:15], v[84:87], v[48:55], v92\n\tv_exp_f32 v59, v59\n\tv_exp_f32 v60, v60\n\tv_exp_f32 v61, v61\n\tv_exp_f32 v62, v62\n\t" \
    "v_exp_f32 v63, v63\n\tv_add_u32 v93, 0x1000, v93\n\ts_setprio 2\n\tv_exp_f32 v32, v32\n\tv_exp_f32 v33, v33\n\tv_smfmac_f32_32x32x32_bf16 v[0:15], v[88:91], v[56:63], v92\n\t" \
    "ds_read_b128 v[68:71], v93 offset:2048\n\tds_read_b128 v[84:87], v94 offset:1024\n\tds_read_b128 v[88:91], v94 offset:1536\n\tv_exp_f32 v34, v34\n\tv_exp_f32 v35, v35\n\t" \
    "v_exp_f32 v36, v36\n\ts_waitcnt lgkmcnt(3)\n\tv_mfma_f32_32x32x16_bf16 v[48:63], v[72:75], v[64:67], v[16:31]\n\tv_exp_f32 v37, v37\n\tv_exp_f32 v38, v38\n\tv_exp_f32 v39, v39\n\t" \
    "v_exp_f32 v40, v40\n\tv_exp_f32 v41, v41\n\tv_exp_f32 v42, v42\n\tv_smfmac_f32_32x32x32_bf16 v[0:15], v[76:79], v[32:39], v92\n\tv_exp_f32 v43, v43\n\tv_exp_f32 v44, v44\n\t" \
    "v_exp_f32 v45, v45\n\tv_exp_f32 v46, v46\n\tv_exp_f32 v47, v47\n\tv_exp_f32 v48, v48\n\tv_exp_f32 v49, v49\n\tv_smfmac_f32_32x32x32_bf16 v[0:15], v[80:83], v[40:47], v92\n\t" \
    "ds_read_b128 v[72:75], v93 offset:3072\n\tds_read_b128 v[76:79], v94 offset:2048\n\tds_read_b128 v[80:83], v94 offset:2560\n\tv_exp_f32 v50, v50\n\tv_exp_f32 v51, v51\n\t" \
    "v_exp_f32 v52, v52\n\ts_waitcnt lgkmcnt(3)\n\tv_mfma_f32_32x32x16_bf16 v[32:47], v[68:71], v[64:67], v[16:31]\n\tv_exp_f32 v53, v53\n\tv_exp_f32 v54, v54\n\tv_exp_f32 v55, v55\n\t" \
    "v_exp_f32 v56, v56\n\tv_exp_f32 v57, v57\n\tv_exp_f32 v58, v58\n\tv_smfmac_f32_32x32x32_bf16 v[0:15], v[84:87], v[48:55], v92\n\tv_exp_f32 v59, v59\n\tv_exp_f32 v60, v60\n\t" \
    "v_exp_f32 v61, v61\n\tv_exp_f32 v62, v62\n\tv_exp_f32 v63, v63\n\ts_setprio 1\n\tv_exp_f32 v32, v32\n\tv_exp_f32 v33, v33\n\t" \
    "v_smfmac_f32_32x32x32_bf16 v[0:15], v[88:91], v[56:63], v92\n\tds_read_b128 v[68:71], v93 offset:4096\n\tds_read_b128 v[84:87], v94 offset:3072\n\t" \
    "ds_read_b128 v[88:91], v94 offset:3584\n\tv_exp_f32 v34, v34\n\tv_exp_f32 v35, v35\n\tv_exp_f32 v36, v36\n\ts_waitcnt lgkmcnt(3)\n\t" \
    "v_mfma_f32_32x32x16_bf16 v[48:63], v[72:75], v[64:67], v[16:31]\n\tv_exp_f32 v37, v37\n\tv_exp_f32 v38, v38\n\tv_exp_f32 v39, v39\n\tv_exp_f32 v40, v40\n\tv_exp_f32 v41, v41\n\t" \
    "v_exp_f32 v42, v42\n\tv_smfmac_f32_32x32x32_bf16 v[0:15], v[76:79], v[32:39], v92\n\tv_exp_f32 v43, v43\n\tv_exp_f32 v44, v44\n\tv_exp_f32 v45, v45\n\tv_exp_f32 v46, v46\n\t" \
    "v_exp_f32 v47, v47\n\tv_exp_f32 v48, v48\n\tv_exp_f32 v49, v49\n\tv_smfmac_f32_32x32x32_bf16 v[0:15], v[80:83], v[40:47], v92\n\tv_add_u32 v94, v94, v95\n\t" \
    "ds_read_b128 v[72:75], v93 offset:5120\n\tds_read_b128 v[76:79], v94 offset:0\n\tds_read_b128 v[80:83], v94 offset:512\n\tv_exp_f32 v50, v50\n\tv_exp_f32 v51, v51\n\t" \
    "v_exp_f32 v52, v52\n\ts_waitcnt lgkmcnt(3)\n\tv_mfma_f32_32x32x16_bf16 v[32:47], v[68:71], v[64:67], v[16:31]\n\tv_exp_f32 v53, v53\n\tv_exp_f32 v54, v54\n\tv_exp_f32 v55, v55\n\t" \
    "v_exp_f32 v56, v56\n\tv_exp_f32 v57, v57\n\tv_exp_f32 v58, v58\n\tv_smfmac_f32_32x32x32_bf16 v[0:15], v[84:87], v[48:55], v92\n\tv_exp_f32 v59, v59\n\tv_exp_f32 v60, v60\n\t" \
    "v_exp_f32 v61, v61\n\tv_exp_f32 v62, v62\n\tv_exp_f32 v63, v63\n\tv_add_u32 v93, 0x1000, v93\n\ts_nop 1\n\tv_smfmac_f32_32x32x32_bf16 v[0:15], v[88:91], v[56:63], v92\n\t" \
    "s_waitcnt lgkmcnt(0)\n\ts_nop 7\n\ts_nop 7\n\ts_mov_b32 m0, s88\n\ts_setprio 0\n\t"
#define ATT_STAGE_B \
    "s_mov_b32 s88, m0\n\ts_bitcmp1_b32 %[flg], 2\n\ts_cselect_b64 vcc, -1, 0\n\ts_mov_b32 s90, 0xe000\n\ts_mov_b32 s91, 0\n\ts_setprio 3\n\tv_mov_b32 v93, %[ka]\n\t" \
    "v_mov_b32 v94, %[va]\n\tv_mov_b32 v95, %[vs]\n\tds_read_b128 v[68:71], v93\n\tds_read_b128 v[72:75], v93 offset:1024\n\tds_read_b128 v[76:79], v94\n\t" \
    "ds_read_b128 v[80:83], v94 offset:512\n\tv_mov_b32 v16, %[negm]\n\tv_mov_b32 v17, %[negm]\n\tv_mov_b32 v18, %[negm]\n\tv_mov_b32 v19, %[negm]\n\tv_mov_b32 v20, %[negm]\n\t" \
    "v_mov_b32 v21, %[negm]\n\tv_mov_b32 v22, %[negm]\n\tv_mov_b32 v23, %[negm]\n\tv_mov_b32 v24, %[negm]\n\tv_mov_b32 v25, %[negm]\n\tv_mov_b32 v26, %[negm]\n\t" \
    "v_mov_b32 v27, %[negm]\n\tv_mov_b32 v28, %[negm]\n\tv_mov_b32 v29, %[negm]\n\tv_mov_b32 v30, %[negm]\n\tv_mov_b32 v31, %[negm]\n\tv_mov_b32 v92, 0xdddddddd\n\t" \
    "s_waitcnt lgkmcnt(3)\n\tv_mfma_f32_32x32x16_bf16 v[32:47], v[68:71], v[64:67], v[16:31]\n\tv_mov_b64 v[0:1], 0\n\tv_mov_b64 v[2:3], 0\n\tv_mov_b64 v[4:5], 0\n\t" \
    "v_mov_b64 v[6:7], 0\n\tv_mov_b64 v[8:9], 0\n\tv_mov_b64 v[10:11], 0\n\tv_mov_b64 v[12:13], 0\n\tv_mov_b64 v[14:15], 0\n\tv_mov_b64 v[56:57], 0\n\tv_mov_b64 v[58:59], 0\n\t" \
    "v_mov_b64 v[60:61], 0\n\tv_mov_b64 v[62:63], 0\n\tv_mov_b64 v[88:89], 0\n\tv_mov_b64 v[90:91], 0\n\ts_waitcnt lgkmcnt(0)\n\tv_exp_f32 v32, v32\n\tv_exp_f32 v33, v33\n\t" \
    "v_smfmac_f32_32x32x32_bf16 v[0:15], v[88:91], v[56:63], v92\n\ts_cbranch_vccz 25f\n\ts_add_u32 s89, %[wld], 0\n\ts_mov_b32 m0, s89\n\ts_nop 0\n\t" \
    "global_load_lds_dwordx4 %[wga], off\n\tv_lshl_add_u64 %[wga], s[90:91], 0, %[wga]\n\t25:\n\tds_read_b128 v[68:71], v93 offset:2048\n\tds_read_b128 v[84:87], v94 offset:1024\n\t" \
    "ds_read_b128 v[88:91], v94 offset:1536\n\tv_exp_f32 v34, v34\n\tv_exp_f32 v35, v35\n\tv_exp_f32 v36, v36\n\ts_waitcnt lgkmcnt(3)\n\t" \
    "v_mfma_f32_32x32x16_bf16 v[48:63], v[72:75], v[64:67], v[16:31]\n\tv_exp_f32 v37, v37\n\tv_exp_f32 v38, v38\n\tv_exp_f32 v39, v39\n\tv_exp_f32 v40, v40\n\tv_exp_f32 v41, v41\n\t" \
    "v_exp_f32 v42, v42\n\tv_smfmac_f32_32x32x32_bf16 v[0:15], v[76:79], v[32:39], v92\n\tv_exp_f32 v43, v43\n\tv_exp_f32 v44, v44\n\tv_exp_f32 v45, v45\n\tv_exp_f32 v46, v46\n\t" \
    "v_exp_f32 v47, v47\n\tv_exp_f32 v48, v48\n\tv_exp_f32 v49, v49\n\tv_smfmac_f32_32x32x32_bf16 v[0:15], v[80:83], v[40:47], v92\n\ts_cbranch_vccz 26f\n\t" \
    "s_add_u32 s89, %[wld], 1024\n\ts_mov_b32 m0, s89\n\ts_nop 0\n\tglobal_load_lds_dwordx4 %[wga], off\n\tv_lshl_add_u64 %[wga], s[90:91], 0, %[wga]\n\t26:\n\t" \
    "ds_read_b128 v[72:75], v93 offset:3072\n\tds_read_b128 v[76:79], v94 offset:2048\n\tds_read_b128 v[80:83], v94 offset:2560\n\tv_exp_f32 v50, v50\n\tv_exp_f32 v51, v51\n\t" \
    "v_exp_f32 v52, v52\n\ts_waitcnt lgkmcnt(3)\n\tv_mfma_f32_32x32x16_bf16 v[32:47], v[68:71], v[64:67], v[16:31]\n\tv_exp_f32 v53, v53\n\tv_exp_f32 v54, v54\n\tv_exp_f32 v55, v55\n\t" \
    "v_exp_f32 v56, v56\n\tv_exp_f32 v57, v57\n\tv_exp_f32 v58, v58\n\tv_smfmac_f32_32x32x32_bf16 v[0:15], v[84:87], v[48:55], v92\n\tv_exp_f32 v59, v59\n\tv_exp_f32 v60, v60\n\t" \
    "v_exp_f32 v61, v61\n\tv_exp_f32 v62, v62\n\tv_exp_f32 v63, v63\n\tv_exp_f32 v32, v32\n\tv_exp_f32 v33, v33\n\tv_smfmac_f32_32x32x32_bf16 v[0:15], v[88:91], v[56:63], v92\n\t" \
    "s_cbranch_vccz 27f\n\ts_add_u32 s89, %[wld], 2048\n\ts_mov_b32 m0, s89\n\ts_nop 0\n\tglobal_load_lds_dwordx4 %[wga], off\n\tv_lshl_add_u64 %[wga], s[90:91], 0, %[wga]\n\t27:\n\t" \
    "ds_read_b128 v[68:71], v93 offset:4096\n\tds_read_b128 v[84:87], v94 offset:3072\n\tds_read_b128 v[88:91], v94 offset:3584\n\tv_exp_f32 v34, v34\n\tv_exp_f32 v35, v35\n\t" \
    "v_exp_f32 v36, v36\n\ts_waitcnt lgkmcnt(3)\n\tv_mfma_f32_32x32x16_bf16 v[48:63], v[72:75], v[64:67], v[16:31]\n\tv_exp_f32 v37, v37\n\tv_exp_f32 v38, v38\n\tv_exp_f32 v39, v39\n\t" \
    "v_exp_f32 v40, v40\n\tv_exp_f32 v41, v41\n\tv_exp_f32 v42, v42\n\tv_smfmac_f32_32x32x32_bf16 v[0:15], v[76:79], v[32:39], v92\n\tv_exp_f32 v43, v43\n\tv_exp_f32 v44, v44\n\t" \
    "v_exp_f32 v45, v45\n\tv_exp_f32 v46, v46\n\tv_exp_f32 v47, v47\n\tv_exp_f32 v48, v48\n\tv_exp_f32 v49, v49\n\tv_smfmac_f32_32x32x32_bf16 v[0:15], v[80:83], v[40:47], v92\n\t" \
    "s_cbranch_vccz 28f\n\ts_add_u32 s89, %[wld], 3072\n\ts_mov_b32 m0, s89\n\ts_nop 0\n\tglobal_load_lds_dwordx4 %[wga], off\n\tv_lshl_add_u64 %[wga], s[90:91], 0, %[wga]\n\t" \
    "s_waitcnt vmcnt(3)\n\tds_read_b64_tr_b16 v[160:161], %[wta] offset:0\n\tds_read_b64_tr_b16 v[162:163], %[wta] offset:512\n\t28:\n\tv_add_u32 v94, v94, v95\n\t" \
    "ds_read_b128 v[72:75], v93 offset:5120\n\tds_read_b128 v[76:79], v94 offset:0\n\tds_read_b128 v[80:83], v94 offset:512\n\tv_exp_f32 v50, v50\n\tv_exp_f32 v51, v51\n\t" \
    "v_exp_f32 v52, v52\n\ts_waitcnt lgkmcnt(3)\n\tv_mfma_f32_32x32x16_bf16 v[32:47], v[68:71], v[64:67], v[16:31]\n\tv_exp_f32 v53, v53\n\tv_exp_f32 v54, v54\n\tv_exp_f32 v55, v55\n\t" \
    "v_exp_f32 v56, v56\n\tv_exp_f32 v57, v57\n\tv_exp_f32 v58, v58\n\tv_smfmac_f32_32x32x32_bf16 v[0:15], v[84:87], v[48:55], v92\n\tv_exp_f32 v59, v59\n\tv_exp_f32 v60, v60\n\t" \
    "v_exp_f32 v61, v61\n\tv_exp_f32 v62, v62\n\tv_exp_f32 v63, v63\n\tv_add_u32 v93, 0x1000, v93\n\tv_exp_f32 v32, v32\n\tv_exp_f32 v33, v33\n\t" \
    "v_smfmac_f32_32x32x32_bf16 v[0:15], v[88:91], v[56:63], v92\n\ts_cbranch_vccz 29f\n\ts_add_u32 s89, %[wld], 0\n\ts_mov_b32 m0, s89\n\ts_nop 0\n\t" \
    "global_load_lds_dwordx4 %[wga], off\n\tv_lshl_add_u64 %[wga], s[90:91], 0, %[wga]\n\ts_waitcnt vmcnt(3)\n\tds_read_b64_tr_b16 v[164:165], %[wta] offset:1024\n\t" \
    "ds_read_b64_tr_b16 v[166:167], %[wta] offset:1536\n\t29:\n\tds_read_b128 v[68:71], v93 offset:2048\n\tds_read_b128 v[84:87], v94 offset:1024\n\t" \
    "ds_read_b128 v[88:91], v94 offset:1536\n\tv_exp_f32 v34, v34\n\tv_exp_f32 v35, v35\n\tv_exp_f32 v36, v36\n\ts_waitcnt lgkmcnt(3)\n\t" \
    "v_mfma_f32_32x32x16_bf16 v[48:63], v[72:75], v[64:67], v[16:31]\n\tv_exp_f32 v37, v37\n\tv_exp_f32 v38, v38\n\tv_exp_f32 v39, v39\n\tv_exp_f32 v40, v40\n\tv_exp_f32 v41, v41\n\t" \
    "v_exp_f32 v42, v42\n\tv_smfmac_f32_32x32x32_bf16 v[0:15], v[76:79], v[32:39], v92\n\tv_exp_f32 v43, v43\n\tv_exp_f32 v44, v44\n\tv_exp_f32 v45, v45\n\tv_exp_f32 v46, v46\n\t" \
    "v_exp_f32 v47, v47\n\tv_exp_f32 v48, v48\n\tv_exp_f32 v49, v49\n\tv_smfmac_f32_32x32x32_bf16 v[0:15], v[80:83], v[40:47], v92\n\ts_cbranch_vccz 30f\n\t" \
    "s_add_u32 s89, %[wld], 1024\n\ts_mov_b32 m0, s89\n\ts_nop 0\n\tglobal_load_lds_dwordx4 %[wga], off\n\tv_lshl_add_u64 %[wga], s[90:91], 0, %[wga]\n\ts_waitcnt vmcnt(3)\n\t" \
    "ds_read_b64_tr_b16 v[168:169], %[wta] offset:2048\n\tds_read_b64_tr_b16 v[170:171], %[wta] offset:2560\n\t30:\n\tds_read_b128 v[72:75], v93 offset:3072\n\t" \
    "ds_read_b128 v[76:79], v94 offset:2048\n\tds_read_b128 v[80:83], v94 offset:2560\n\tv_exp_f32 v50, v50\n\tv_exp_f32 v51, v51\n\tv_exp_f32 v52, v52\n\ts_waitcnt lgkmcnt(3)\n\t" \
    "v_mfma_f32_32x32x16_bf16 v[32:47], v[68:71], v[64:67], v[16:31]\n\tv_exp_f32 v53, v53\n\tv_exp_f32 v54, v54\n\tv_exp_f32 v55, v55\n\tv_exp_f32 v56, v56\n\tv_exp_f32 v57, v57\n\t" \
    "v_exp_f32 v58, v58\n\tv_smfmac_f32_32x32x32_bf16 v[0:15], v[84:87], v[48:55], v92\n\tv_exp_f32 v59, v59\n\tv_exp_f32 v60, v60\n\tv_exp_f32 v61, v61\n\tv_exp_f32 v62, v62\n\t" \
    "v_exp_f32 v63, v63\n\tv_exp_f32 v32, v32\n\tv_exp_f32 v33, v33\n\tv_smfmac_f32_32x32x32_bf16 v[0:15], v[88:91], v[56:63], v92\n\ts_cbranch_vccz 31f\n\t" \
    "s_add_u32 s89, %[wld], 2048\n\ts_mov_b32 m0, s89\n\ts_nop 0\n\tglobal_load_lds_dwordx4 %[wga], off\n\tv_lshl_add_u64 %[wga], s[90:91], 0, %[wga]\n\ts_waitcnt vmcnt(3)\n\t" \
    "ds_read_b64_tr_b16 v[172:173], %[wta] offset:3072\n\tds_read_b64_tr_b16 v[174:175], %[wta] offset:3584\n\t31:\n\tds_read_b128 v[68:71], v93 offset:4096\n\t" \
    "ds_read_b128 v[84:87], v94 offset:3072\n\tds_read_b128 v[88:91], v94 offset:3584\n\tv_exp_f32 v34, v34\n\tv_exp_f32 v35, v35\n\tv_exp_f32 v36, v36\n\ts_waitcnt lgkmcnt(3)\n\t" \
    "v_mfma_f32_32x32x16_bf16 v[48:63], v[72:75], v[64:67], v[16:31]\n\tv_exp_f32 v37, v37\n\tv_exp_f32 v38, v38\n\tv_exp_f32 v39, v39\n\tv_exp_f32 v40, v40\n\tv_exp_f32 v41, v41\n\t" \
    "v_exp_f32 v42, v42\n\tv_smfmac_f32_32x32x32_bf16 v[0:15], v[76:79], v[32:39], v92\n\tv_exp_f32 v43, v43\n\tv_exp_f32 v44, v44\n\tv_exp_f32 v45, v45\n\tv_exp_f32 v46, v46\n\t" \
    "v_exp_f32 v47, v47\n\tv_exp_f32 v48, v48\n\tv_exp_f32 v49, v49\n\tv_smfmac_f32_32x32x32_bf16 v[0:15], v[80:83], v[40:47], v92\n\ts_cbranch_vccz 32f\n\t" \
    "s_add_u32 s89, %[wld], 3072\n\ts_mov_b32 m0, s89\n\ts_nop 0\n\tglobal_load_lds_dwordx4 %[wga], off\n\tv_lshl_add_u64 %[wga], s[90:91], 0, %[wga]\n\ts_waitcnt vmcnt(3)\n\t" \
    "ds_read_b64_tr_b16 v[176:177], %[wta] offset:0\n\tds_read_b64_tr_b16 v[178:179], %[wta] offset:512\n\t32:\n\tv_add_u32 v94, v94, v95\n\tds_read_b128 v[72:75], v93 offset:5120\n\t" \
    "ds_read_b128 v[76:79], v94 offset:0\n\tds_read_b128 v[80:83], v94 offset:512\n\tv_exp_f32 v50, v50\n\tv_exp_f32 v51, v51\n\tv_exp_f32 v52, v52\n\ts_waitcnt lgkmcnt(3)\n\t" \
    "v_mfma_f32_32x32x16_bf16 v[32:47], v[68:71], v[64:67], v[16:31]\n\tv_exp_f32 v53, v53\n\tv_exp_f32 v54, v54\n\tv_exp_f32 v55, v55\n\tv_exp_f32 v56, v56\n\tv_exp_f32 v57, v57\n\t" \
    "v_exp_f32 v58, v58\n\tv_smfmac_f32_32x32x32_bf16 v[0:15], v[84:87], v[48:55], v92\n\tv_exp_f32 v59, v59\n\tv_exp_f32 v60, v60\n\tv_exp_f32 v61, v61\n\tv_exp_f32 v62, v62\n\t" \
    "v_exp_f32 v63, v63\n\tv_add_u32 v93, 0x1000, v93\n\tv_exp_f32 v32, v32\n\tv_exp_f32 v33, v33\n\tv_smfmac_f32_32x32x32_bf16 v[0:15], v[88:91], v[56:63], v92\n\t" \
    "s_cbranch_vccz 33f\n\ts_add_u32 s89, %[wld], 0\n\ts_mov_b32 m0, s89\n\ts_nop 0\n\tglobal_load_lds_dwordx4 %[wga], off\n\tv_lshl_add_u64 %[wga], s[90:91], 0, %[wga]\n\t" \
    "s_waitcnt vmcnt(3)\n\tds_read_b64_tr_b16 v[180:181], %[wta] offset:1024\n\tds_read_b64_tr_b16 v[182:183], %[wta] offset:1536\n\t33:\n\tds_read_b128 v[68:71], v93 offset:2048\n\t" \
    "ds_read_b128 v[84:87], v94 offset:1024\n\tds_read_b128 v[88:91], v94 offset:1536\n\tv_exp_f32 v34, v34\n\tv_exp_f32 v35, v35\n\tv_exp_f32 v36, v36\n\ts_waitcnt lgkmcnt(3)\n\t" \
    "v_mfma_f32_32x32x16_bf16 v[48:63], v[72:75], v[64:67], v[16:31]\n\tv_exp_f32 v37, v37\n\tv_exp_f32 v38, v38\n\tv_exp_f32 v39, v39\n\tv_exp_f32 v40, v40\n\tv_exp_f32 v41, v41\n\t" \
    "v_exp_f32 v42, v42\n\tv_smfmac_f32_32x32x32_bf16 v[0:15], v[76:79], v[32:39], v92\n\tv_exp_f32 v43, v43\n\tv_exp_f32 v44, v44\n\tv_exp_f32 v45, v45\n\tv_exp_f32 v46, v46\n\t" \
    "v_exp_f32 v47, v47\n\tv_exp_f32 v48, v48\n\tv_exp_f32 v49, v49\n\tv_smfmac_f32_32x32x32_bf16 v[0:15], v[80:83], v[40:47], v92\n\ts_cbranch_vccz 34f\n\t" \
    "s_add_u32 s89, %[wld], 1024\n\ts_mov_b32 m0, s89\n\ts_nop 0\n\tglobal_load_lds_dwordx4 %[wga], off\n\tv_lshl_add_u64 %[wga], s[90:91], 0, %[wga]\n\ts_waitcnt vmcnt(3)\n\t" \
    "ds_read_b64_tr_b16 v[184:185], %[wta] offset:2048\n\tds_read_b64_tr_b16 v[186:187], %[wta] offset:2560\n\t34:\n\tds_read_b128 v[72:75], v93 offset:3072\n\t" \
    "ds_read_b128 v[76:79], v94 offset:2048\n\tds_read_b128 v[80:83], v94 offset:2560\n\tv_exp_f32 v50, v50\n\tv_exp_f32 v51, v51\n\tv_exp_f32 v52, v52\n\ts_waitcnt lgkmcnt(3)\n\t" \
    "v_mfma_f32_32x32x16_bf16 v[32:47], v[68:71], v[64:67], v[16:31]\n\tv_exp_f32 v53, v53\n\tv_exp_f32 v54, v54\n\tv_exp_f32 v55, v55\n\tv_exp_f32 v56, v56\n\tv_exp_f32 v57, v57\n\t" \
    "v_exp_f32 v58, v58\n\tv_smfmac_f32_32x32x32_bf16 v[0:15], v[84:87], v[48:55], v92\n\tv_exp_f32 v59, v59\n\tv_exp_f32 v60, v60\n\tv_exp_f32 v61, v61\n\tv_exp_f32 v62, v62\n\t" \
    "v_exp_f32 v63, v63\n\tv_exp_f32 v32, v32\n\tv_exp_f32 v33, v33\n\tv_smfmac_f32_32x32x32_bf16 v[0:15], v[88:91], v[56:63], v92\n\ts_cbranch_vccz 35f\n\t" \
    "s_add_u32 s89, %[wld], 2048\n\ts_mov_b32 m0, s89\n\ts_nop 0\n\tglobal_load_lds_dwordx4 %[wga], off\n\tv_lshl_add_u64 %[wga], s[90:91], 0, %[wga]\n\ts_waitcnt vmcnt(3)\n\t" \
    "ds_read_b64_tr_b16 v[188:189], %[wta] offset:3072\n\tds_read_b64_tr_b16 v[190:191], %[wta] offset:3584\n\t35:\n\tds_read_b128 v[68:71], v93 offset:4096\n\t" \
    "ds_read_b128 v[84:87], v94 offset:3072\n\tds_read_b128 v[88:91], v94 offset:3584\n\tv_exp_f32 v34, v34\n\tv_exp_f32 v35, v35\n\tv_exp_f32 v36, v36\n\ts_waitcnt lgkmcnt(3)\n\t" \
    "v_mfma_f32_32x32x16_bf16 v[48:63], v[72:75], v[64:67], v[16:31]\n\tv_exp_f32 v37, v37\n\tv_exp_f32 v38, v38\n\tv_exp_f32 v39, v39\n\tv_exp_f32 v40, v40\n\tv_exp_f32 v41, v41\n\t" \
    "v_exp_f32 v42, v42\n\tv_smfmac_f32_32x32x32_bf16 v[0:15], v[76:79], v[32:39], v92\n\tv_exp_f32 v43, v43\n\tv_exp_f32 v44, v44\n\tv_exp_f32 v45, v45\n\tv_exp_f32 v46, v46\n\t" \
    "v_exp_f32 v47, v47\n\tv_exp_f32 v48, v48\n\tv_exp_f32 v49, v49\n\tv_smfmac_f32_32x32x32_bf16 v[0:15], v[80:83], v[40:47], v92\n\ts_cbranch_vccz 36f\n\t" \
    "s_add_u32 s89, %[wld], 3072\n\ts_mov_b32 m0, s89\n\ts_nop 0\n\tglobal_load_lds_dwordx4 %[wga], off\n\tv_lshl_add_u64 %[wga], s[90:91], 0, %[wga]\n\ts_waitcnt vmcnt(3)\n\t" \
    "ds_read_b64_tr_b16 v[192:193], %[wta] offset:0\n\tds_read_b64_tr_b16 v[194:195], %[wta] offset:512\n\t36:\n\tv_add_u32 v94, v94, v95\n\tds_read_b128 v[72:75], v93 offset:5120\n\t" \
    "ds_read_b128 v[76:79], v94 offset:0\n\tds_read_b128 v[80:83], v94 offset:512\n\tv_exp_f32 v50, v50\n\tv_exp_f32 v51, v51\n\tv_exp_f32 v52, v52\n\ts_waitcnt lgkmcnt(3)\n\t" \
    "v_mfma_f32_32x32x16_bf16 v[32:47], v[68:71], v[64:67], v[16:31]\n\tv_exp_f32 v53, v53\n\tv_exp_f32 v54, v54\n\tv_exp_f32 v55, v55\n\tv_exp_f32 v56, v56\n\tv_exp_f32 v57, v57\n\t" \
    "v_exp_f32 v58, v58\n\tv_smfmac_f32_32x32x32_bf16 v[0:15], v[84:87], v[48:55], v92\n\tv_exp_f32 v59, v59\n\tv_exp_f32 v60, v60\n\tv_exp_f32 v61, v61\n\tv_exp_f32 v62, v62\n\t" \
    "v_exp_f32 v63, v63\n\tv_add_u32 v93, 0x1000, v93\n\tv_exp_f32 v32, v32\n\tv_exp_f32 v33, v33\n\tv_smfmac_f32_32x32x32_bf16 v[0:15], v[88:91], v[56:63], v92\n\t" \
    "s_cbranch_vccz 37f\n\ts_add_u32 s89, %[wld], 0\n\ts_mov_b32 m0, s89\n\ts_nop 0\n\tglobal_load_lds_dwordx4 %[wga], off\n\tv_lshl_add_u64 %[wga], s[90:91], 0, %[wga]\n\t" \
    "s_waitcnt vmcnt(3)\n\tds_read_b64_tr_b16 v[196:197], %[wta] offset:1024\n\tds_read_b64_tr_b16 v[198:199], %[wta] offset:1536\n\t37:\n\tds_read_b128 v[68:71], v93 offset:2048\n\t" \
    "ds_read_b128 v[84:87], v94 offset:1024\n\tds_read_b128 v[88:91], v94 offset:1536\n\tv_exp_f32 v34, v34\n\tv_exp_f32 v35, v35\n\tv_exp_f32 v36, v36\n\ts_waitcnt lgkmcnt(3)\n\t" \
    "v_mfma_f32_32x32x16_bf16 v[48:63], v[72:75], v[64:67], v[16:31]\n\tv_exp_f32 v37, v37\n\tv_exp_f32 v38, v38\n\tv_exp_f32 v39, v39\n\tv_exp_f32 v40, v40\n\tv_exp_f32 v41, v41\n\t" \
    "v_exp_f32 v42, v42\n\tv_smfmac_f32_32x32x32_bf16 v[0:15], v[76:79], v[32:39], v92\n\tv_exp_f32 v43, v43\n\tv_exp_f32 v44, v44\n\tv_exp_f32 v45, v45\n\tv_exp_f32 v46, v46\n\t" \
    "v_exp_f32 v47, v47\n\tv_exp_f32 v48, v48\n\tv_exp_f32 v49, v49\n\tv_smfmac_f32_32x32x32_bf16 v[0:15], v[80:83], v[40:47], v92\n\ts_cbranch_vccz 38f\n\t" \
    "s_add_u32 s89, %[wld], 1024\n\ts_mov_b32 m0, s89\n\ts_nop 0\n\tglobal_load_lds_dwordx4 %[wga], off\n\tv_lshl_add_u64 %[wga], s[90:91], 0, %[wga]\n\ts_waitcnt vmcnt(3)\n\t" \
    "ds_read_b64_tr_b16 v[200:201], %[wta] offset:2048\n\tds_read_b64_tr_b16 v[202:203], %[wta] offset:2560\n\t38:\n\tds_read_b128 v[72:75], v93 offset:3072\n\t" \
    "ds_read_b128 v[76:79], v94 offset:2048\n\tds_read_b128 v[80:83], v94 offset:2560\n\tv_exp_f32 v50, v50\n\tv_exp_f32 v51, v51\n\tv_exp_f32 v52, v52\n\ts_waitcnt lgkmcnt(3)\n\t" \
    "v_mfma_f32_32x32x16_bf16 v[32:47], v[68:71], v[64:67], v[16:31]\n\tv_exp_f32 v53, v53\n\tv_exp_f32 v54, v54\n\tv_exp_f32 v55, v55\n\tv_exp_f32 v56, v56\n\tv_exp_f32 v57, v57\n\t" \
    "v_exp_f32 v58, v58\n\tv_smfmac_f32_32x32x32_bf16 v[0:15], v[84:87], v[48:55], v92\n\tv_exp_f32 v59, v59\n\tv_exp_f32 v60, v60\n\tv_exp_f32 v61, v61\n\tv_exp_f32 v62, v62\n\t" \
    "v_exp_f32 v63, v63\n\ts_setprio 2\n\tv_exp_f32 v32, v32\n\tv_exp_f32 v33, v33\n\tv_smfmac_f32_32x32x32_bf16 v[0:15], v[88:91], v[56:63], v92\n\ts_cbranch_vccz 39f\n\t" \
    "s_add_u32 s89, %[wld], 2048\n\ts_mov_b32 m0, s89\n\ts_nop 0\n\tglobal_load_lds_dwordx4 %[wga], off\n\tv_lshl_add_u64 %[wga], s[90:91], 0, %[wga]\n\ts_waitcnt vmcnt(3)\n\t" \
    "ds_read_b64_tr_b16 v[204:205], %[wta] offset:3072\n\tds_read_b64_tr_b16 v[206:207], %[wta] offset:3584\n\t39:\n\tds_read_b128 v[68:71], v93 offset:4096\n\t" \
    "ds_read_b128 v[84:87], v94 offset:3072\n\tds_read_b128 v[88:91], v94 offset:3584\n\tv_exp_f32 v34, v34\n\tv_exp_f32 v35, v35\n\tv_exp_f32 v36, v36\n\ts_waitcnt lgkmcnt(3)\n\t" \
    "v_mfma_f32_32x32x16_bf16 v[48:63], v[72:75], v[64:67], v[16:31]\n\tv_exp_f32 v37, v37\n\tv_exp_f32 v38, v38\n\tv_exp_f32 v39, v39\n\tv_exp_f32 v40, v40\n\tv_exp_f32 v41, v41\n\t" \
    "v_exp_f32 v42, v42\n\tv_smfmac_f32_32x32x32_bf16 v[0:15], v[76:79], v[32:39], v92\n\tv_exp_f32 v43, v43\n\tv_exp_f32 v44, v44\n\tv_exp_f32 v45, v45\n\tv_exp_f32 v46, v46\n\t" \
    "v_exp_f32 v47, v47\n\tv_exp_f32 v48, v48\n\tv_exp_f32 v49, v49\n\tv_smfmac_f32_32x32x32_bf16 v[0:15], v[80:83], v[40:47], v92\n\ts_cbranch_vccz 40f\n\t" \
    "s_add_u32 s89, %[wld], 3072\n\ts_mov_b32 m0, s89\n\ts_nop 0\n\tglobal_load_lds_dwordx4 %[wga], off\n\tv_lshl_add_u64 %[wga], s[90:91], 0, %[wga]\n\ts_waitcnt vmcnt(3)\n\t" \
    "ds_read_b64_tr_b16 v[208:209], %[wta] offset:0\n\tds_read_b64_tr_b16 v[210:211], %[wta] offset:512\n\t40:\n\tv_add_u32 v94, v94, v95\n\tds_read_b128 v[72:75], v93 offset:5120\n\t" \
    "ds_read_b128 v[76:79], v94 offset:0\n\tds_read_b128 v[80:83], v94 offset:512\n\tv_exp_f32 v50, v50\n\tv_exp_f32 v51, v51\n\tv_exp_f32 v52, v52\n\ts_waitcnt lgkmcnt(3)\n\t" \
    "v_mfma_f32_32x32x16_bf16 v[32:47], v[68:71], v[64:67], v[16:31]\n\tv_exp_f32 v53, v53\n\tv_exp_f32 v54, v54\n\tv_exp_f32 v55, v55\n\tv_exp_f32 v56, v56\n\tv_exp_f32 v57, v57\n\t" \
    "v_exp_f32 v58, v58\n\tv_smfmac_f32_32x32x32_bf16 v[0:15], v[84:87], v[48:55], v92\n\tv_exp_f32 v59, v59\n\tv_exp_f32 v60, v60\n\tv_exp_f32 v61, v61\n\tv_exp_f32 v62, v62\n\t" \
    "v_exp_f32 v63, v63\n\tv_add_u32 v93, 0x1000, v93\n\tv_exp_f32 v32, v32\n\tv_exp_f32 v33, v33\n\tv_smfmac_f32_32x32x32_bf16 v[0:15], v[88:91], v[56:63], v92\n\t" \
    "s_cbranch_vccz 41f\n\ts_waitcnt vmcnt(2)\n\tds_read_b64_tr_b16 v[212:213], %[wta] offset:1024\n\tds_read_b64_tr_b16 v[214:215], %[wta] offset:1536\n\t41:\n\t" \
    "ds_read_b128 v[68:71], v93 offset:2048\n\tds_read_b128 v[84:87], v94 offset:1024\n\tds_read_b128 v[88:91], v94 offset:1536\n\tv_exp_f32 v34, v34\n\tv_exp_f32 v35, v35\n\t" \
    "v_exp_f32 v36, v36\n\ts_waitcnt lgkmcnt(3)\n\tv_mfma_f32_32x32x16_bf16 v[48:63], v[72:75], v[64:67], v[16:31]\n\tv_exp_f32 v37, v37\n\tv_exp_f32 v38, v38\n\tv_exp_f32 v39, v39\n\t" \
    "v_exp_f32 v40, v40\n\tv_exp_f32 v41, v41\n\tv_exp_f32 v42, v42\n\tv_smfmac_f32_32x32x32_bf16 v[0:15], v[76:79], v[32:39], v92\n\tv_exp_f32 v43, v43\n\tv_exp_f32 v44, v44\n\t" \
    "v_exp_f32 v45, v45\n\tv_exp_f32 v46, v46\n\tv_exp_f32 v47, v47\n\tv_exp_f32 v48, v48\n\tv_exp_f32 v49, v49\n\tv_smfmac_f32_32x32x32_bf16 v[0:15], v[80:83], v[40:47], v92\n\t" \
    "s_cbranch_vccz 42f\n\ts_waitcnt vmcnt(1)\n\tds_read_b64_tr_b16 v[216:217], %[wta] offset:2048\n\tds_read_b64_tr_b16 v[218:219], %[wta] offset:2560\n\t42:\n\t" \
    "ds_read_b128 v[72:75], v93 offset:3072\n\tds_read_b128 v[76:79], v94 offset:2048\n\tds_read_b128 v[80:83], v94 offset:2560\n\tv_exp_f32 v50, v50\n\tv_exp_f32 v51, v51\n\t" \
    "v_exp_f32 v52, v52\n\ts_waitcnt lgkmcnt(3)\n\tv_mfma_f32_32x32x16_bf16 v[32:47], v[68:71], v[64:67], v[16:31]\n\tv_exp_f32 v53, v53\n\tv_exp_f32 v54, v54\n\tv_exp_f32 v55, v55\n\t" \
    "v_exp_f32 v56, v56\n\tv_exp_f32 v57, v57\n\tv_exp_f32 v58, v58\n\tv_smfmac_f32_32x32x32_bf16 v[0:15], v[84:87], v[48:55], v92\n\tv_exp_f32 v59, v59\n\tv_exp_f32 v60, v60\n\t" \
    "v_exp_f32 v61, v61\n\tv_exp_f32 v62, v62\n\tv_exp_f32 v63, v63\n\ts_setprio 1\n\tv_exp_f32 v32, v32\n\tv_exp_f32 v33, v33\n\t" \
    "v_smfmac_f32_32x32x32_bf16 v[0:15], v[88:91], v[56:63], v92\n\ts_cbranch_vccz 43f\n\ts_waitcnt vmcnt(0)\n\tds_read_b64_tr_b16 v[220:221], %[wta] offset:3072\n\t" \
    "ds_read_b64_tr_b16 v[222:223], %[wta] offset:3584\n\t43:\n\tds_read_b128 v[68:71], v93 offset:4096\n\tds_read_b128 v[84:87], v94 offset:3072\n\t" \
    "ds_read_b128 v[88:91], v94 offset:3584\n\tv_exp_f32 v34, v34\n\tv_exp_f32 v35, v35\n\tv_exp_f32 v36, v36\n\ts_waitcnt lgkmcnt(3)\n\t" \
    "v_mfma_f32_32x32x16_bf16 v[48:63], v[72:75], v[64:67], v[16:31]\n\tv_exp_f32 v37, v37\n\tv_exp_f32 v38, v38\n\tv_exp_f32 v39, v39\n\tv_exp_f32 v40, v40\n\tv_exp_f32 v41, v41\n\t" \
    "v_exp_f32 v42, v42\n\tv_smfmac_f32_32x32x32_bf16 v[0:15], v[76:79], v[32:39], v92\n\tv_exp_f32 v43, v43\n\tv_exp_f32 v44, v44\n\tv_exp_f32 v45, v45\n\tv_exp_f32 v46, v46\n\t" \
    "v_exp_f32 v47, v47\n\tv_exp_f32 v48, v48\n\tv_exp_f32 v49, v49\n\tv_smfmac_f32_32x32x32_bf16 v[0:15], v[80:83], v[40:47], v92\n\tv_add_u32 v94, v94, v95\n\t" \
    "ds_read_b128 v[72:75], v93 offset:5120\n\tds_read_b128 v[76:79], v94 offset:0\n\tds_read_b128 v[80:83], v94 offset:512\n\tv_exp_f32 v50, v50\n\tv_exp_f32 v51, v51\n\t" \
    "v_exp_f32 v52, v52\n\ts_waitcnt lgkmcnt(3)\n\tv_mfma_f32_32x32x16_bf16 v[32:47], v[68:71], v[64:67], v[16:31]\n\tv_exp_f32 v53, v53\n\tv_exp_f32 v54, v54\n\tv_exp_f32 v55, v55\n\t" \
    "v_exp_f32 v56, v56\n\tv_exp_f32 v57, v57\n\tv_exp_f32 v58, v58\n\tv_smfmac_f32_32x32x32_bf16 v[0:15], v[84:87], v[48:55], v92\n\tv_exp_f32 v59, v59\n\tv_exp_f32 v60, v60\n\t" \
    "v_exp_f32 v61, v61\n\tv_exp_f32 v62, v62\n\tv_exp_f32 v63, v63\n\tv_add_u32 v93, 0x1000, v93\n\ts_barrier\n\ts_setprio 3\n\ts_mov_b64 s[86:87], %[wos]\n\ts_mov_b32 s89, %[wod]\n\t" \
    "s_cbranch_vccnz 44f\n\tv_mov_b32 v224, 1\n\tv_lshlrev_b32 v225, 6, %[l16]\n\ts_mov_b64 exec, 0xff\n\tbuffer_atomic_add v224, v225, %[rs], %[cso] offen\n\ts_mov_b64 exec, -1\n\t" \
    "44:\n\tv_exp_f32 v32, v32\n\tv_exp_f32 v33, v33\n\tv_smfmac_f32_32x32x32_bf16 v[0:15], v[88:91], v[56:63], v92\n\ts_mov_b32 m0, s89\n\ts_add_u32 s89, s89, 1024\n\t" \
    "global_load_lds_dwordx4 %[xv], s[86:87]\n\ts_add_u32 s86, s86, 0x2000\n\ts_addc_u32 s87, s87, 0\n\ts_mov_b32 m0, %[xd]\n\ts_nop 0\n\tglobal_load_lds_dwordx4 %[xv], %[xs]\n\t" \
    "ds_read_b128 v[68:71], v93 offset:2048\n\tds_read_b128 v[84:87], v94 offset:1024\n\tds_read_b128 v[88:91], v94 offset:1536\n\tv_exp_f32 v34, v34\n\tv_exp_f32 v35, v35\n\t" \
    "v_exp_f32 v36, v36\n\ts_waitcnt lgkmcnt(3)\n\tv_mfma_f32_32x32x16_bf16 v[48:63], v[72:75], v[64:67], v[16:31]\n\tv_exp_f32 v37, v37\n\tv_exp_f32 v38, v38\n\tv_exp_f32 v39, v39\n\t" \
    "v_exp_f32 v40, v40\n\tv_exp_f32 v41, v41\n\tv_exp_f32 v42, v42\n\tv_smfmac_f32_32x32x32_bf16 v[0:15], v[76:79], v[32:39], v92\n\tv_exp_f32 v43, v43\n\tv_exp_f32 v44, v44\n\t" \
    "v_exp_f32 v45, v45\n\tv_exp_f32 v46, v46\n\tv_exp_f32 v47, v47\n\tv_exp_f32 v48, v48\n\tv_exp_f32 v49, v49\n\tv_smfmac_f32_32x32x32_bf16 v[0:15], v[80:83], v[40:47], v92\n\t" \
    "s_mov_b32 m0, s89\n\ts_add_u32 s89, s89, 1024\n\tglobal_load_lds_dwordx4 %[xv], s[86:87]\n\ts_add_u32 s86, s86, 0x2000\n\ts_addc_u32 s87, s87, 0\n\ts_mov_b32 m0, %[snk]\n\t" \
    "s_add_u32 s90, %[wdo], 0\n\tbuffer_load_dwordx4 %[l16], %[rd], s90 offen lds\n\tds_read_b128 v[72:75], v93 offset:3072\n\tds_read_b128 v[76:79], v94 offset:2048\n\t" \
    "ds_read_b128 v[80:83], v94 offset:2560\n\tv_exp_f32 v50, v50\n\tv_exp_f32 v51, v51\n\tv_exp_f32 v52, v52\n\ts_waitcnt lgkmcnt(3)\n\t" \
    "v_mfma_f32_32x32x16_bf16 v[32:47], v[68:71], v[64:67], v[16:31]\n\tv_exp_f32 v53, v53\n\tv_exp_f32 v54, v54\n\tv_exp_f32 v55, v55\n\tv_exp_f32 v56, v56\n\tv_exp_f32 v57, v57\n\t" \
    "v_exp_f32 v58, v58\n\tv_smfmac_f32_32x32x32_bf16 v[0:15], v[84:87], v[48:55], v92\n\tv_exp_f32 v59, v59\n\tv_exp_f32 v60, v60\n\tv_exp_f32 v61, v61\n\tv_exp_f32 v62, v62\n\t" \
    "v_exp_f32 v63, v63\n\tv_exp_f32 v32, v32\n\tv_exp_f32 v33, v33\n\tv_smfmac_f32_32x32x32_bf16 v[0:15], v[88:91], v[56:63], v92\n\ts_mov_b32 m0, s89\n\ts_add_u32 s89, s89, 1024\n\t" \
    "global_load_lds_dwordx4 %[xv], s[86:87]\n\ts_add_u32 s86, s86, 0x2000\n\ts_addc_u32 s87, s87, 0\n\ts_mov_b32 m0, %[snk]\n\ts_add_u32 s90, %[wdo], 1024\n\t" \
    "buffer_load_dwordx4 %[l16], %[rd], s90 offen lds\n\tds_read_b128 v[68:71], v93 offset:4096\n\tds_read_b128 v[84:87], v94 offset:3072\n\tds_read_b128 v[88:91], v94 offset:3584\n\t" \
    "v_exp_f32 v34, v34\n\tv_exp_f32 v35, v35\n\tv_exp_f32 v36, v36\n\ts_waitcnt lgkmcnt(3)\n\tv_mfma_f32_32x32x16_bf16 v[48:63], v[72:75], v[64:67], v[16:31]\n\tv_exp_f32 v37, v37\n\t" \
    "v_exp_f32 v38, v38\n\tv_exp_f32 v39, v39\n\tv_exp_f32 v40, v40\n\tv_exp_f32 v41, v41\n\tv_exp_f32 v42, v42\n\tv_smfmac_f32_32x32x32_bf16 v[0:15], v[76:79], v[32:39], v92\n\t" \
    "v_exp_f32 v43, v43\n\tv_exp_f32 v44, v44\n\tv_exp_f32 v45, v45\n\tv_exp_f32 v46, v46\n\tv_exp_f32 v47, v47\n\tv_exp_f32 v48, v48\n\tv_exp_f32 v49, v49\n\t" \
    "v_smfmac_f32_32x32x32_bf16 v[0:15], v[80:83], v[40:47], v92\n\ts_mov_b32 m0, s89\n\ts_add_u32 s89, s89, 62464\n\tglobal_load_lds_dwordx4 %[xv], s[86:87]\n\t" \
    "s_add_u32 s86, s86, 0x2000\n\ts_addc_u32 s87, s87, 0\n\ts_mov_b32 m0, %[snk]\n\ts_add_u32 s90, %[wdo], 2048\n\tbuffer_load_dwordx4 %[l16], %[rd], s90 offen lds\n\t" \
    "v_add_u32 v94, v94, v95\n\tds_read_b128 v[72:75], v93 offset:5120\n\tds_read_b128 v[76:79], v94 offset:0\n\tds_read_b128 v[80:83], v94 offset:512\n\tv_exp_f32 v50, v50\n\t" \
    "v_exp_f32 v51, v51\n\tv_exp_f32 v52, v52\n\ts_waitcnt lgkmcnt(3)\n\tv_mfma_f32_32x32x16_bf16 v[32:47], v[68:71], v[64:67], v[16:31]\n\tv_exp_f32 v53, v53\n\tv_exp_f32 v54, v54\n\t" \
    "v_exp_f32 v55, v55\n\tv_exp_f32 v56, v56\n\tv_exp_f32 v57, v57\n\tv_exp_f32 v58, v58\n\tv_smfmac_f32_32x32x32_bf16 v[0:15], v[84:87], v[48:55], v92\n\tv_exp_f32 v59, v59\n\t" \
    "v_exp_f32 v60, v60\n\tv_exp_f32 v61, v61\n\tv_exp_f32 v62, v62\n\tv_exp_f32 v63, v63\n\tv_add_u32 v93, 0x1000, v93\n\tv_exp_f32 v32, v32\n\tv_exp_f32 v33, v33\n\t" \
    "v_smfmac_f32_32x32x32_bf16 v[0:15], v[88:91], v[56:63], v92\n\ts_mov_b32 m0, s89\n\ts_add_u32 s89, s89, 1024\n\tglobal_load_lds_dwordx4 %[xv], s[86:87]\n\t" \
    "s_add_u32 s86, s86, 0x2000\n\ts_addc_u32 s87, s87, 0\n\ts_mov_b32 m0, %[snk]\n\ts_add_u32 s90, %[wdo], 3072\n\tbuffer_load_dwordx4 %[l16], %[rd], s90 offen lds\n\t" \
    "ds_read_b128 v[68:71], v93 offset:2048\n\tds_read_b128 v[84:87], v94 offset:1024\n\tds_read_b128 v[88:91], v94 offset:1536\n\tv_exp_f32 v34, v34\n\tv_exp_f32 v35, v35\n\t" \
    "v_exp_f32 v36, v36\n\ts_waitcnt lgkmcnt(3)\n\tv_mfma_f32_32x32x16_bf16 v[48:63], v[72:75], v[64:67], v[16:31]\n\tv_exp_f32 v37, v37\n\tv_exp_f32 v38, v38\n\tv_exp_f32 v39, v39\n\t" \
    "v_exp_f32 v40, v40\n\tv_exp_f32 v41, v41\n\tv_exp_f32 v42, v42\n\tv_smfmac_f32_32x32x32_bf16 v[0:15], v[76:79], v[32:39], v92\n\tv_exp_f32 v43, v43\n\tv_exp_f32 v44, v44\n\t" \
    "v_exp_f32 v45, v45\n\tv_exp_f32 v46, v46\n\tv_exp_f32 v47, v47\n\tv_exp_f32 v48, v48\n\tv_exp_f32 v49, v49\n\tv_smfmac_f32_32x32x32_bf16 v[0:15], v[80:83], v[40:47], v92\n\t" \
    "s_mov_b32 m0, s89\n\ts_add_u32 s89, s89, 1024\n\tglobal_load_lds_dwordx4 %[xv], s[86:87]\n\ts_add_u32 s86, s86, 0x2000\n\ts_addc_u32 s87, s87, 0\n\ts_mov_b32 m0, %[snk]\n\t" \
    "s_add_u32 s90, %[wdo], 4096\n\tbuffer_load_dwordx4 %[l16], %[rd], s90 offen lds\n\tds_read_b128 v[72:75], v93 offset:3072\n\tds_read_b128 v[76:79], v94 offset:2048\n\t" \
    "ds_read_b128 v[80:83], v94 offset:2560\n\tv_exp_f32 v50, v50\n\tv_exp_f32 v51, v51\n\tv_exp_f32 v52, v52\n\ts_waitcnt lgkmcnt(3)\n\t" \
    "v_mfma_f32_32x32x16_bf16 v[32:47], v[68:71], v[64:67], v[16:31]\n\tv_exp_f32 v53, v53\n\tv_exp_f32 v54, v54\n\tv_exp_f32 v55, v55\n\tv_exp_f32 v56, v56\n\tv_exp_f32 v57, v57\n\t" \
    "v_exp_f32 v58, v58\n\tv_smfmac_f32_32x32x32_bf16 v[0:15], v[84:87], v[48:55], v92\n\tv_exp_f32 v59, v59\n\tv_exp_f32 v60, v60\n\tv_exp_f32 v61, v61\n\tv_exp_f32 v62, v62\n\t" \
    "v_exp_f32 v63, v63\n\tv_exp_f32 v32, v32\n\tv_exp_f32 v33, v33\n\tv_smfmac_f32_32x32x32_bf16 v[0:15], v[88:91], v[56:63], v92\n\ts_mov_b32 m0, s89\n\ts_add_u32 s89, s89, 1024\n\t" \
    "global_load_lds_dwordx4 %[xv], s[86:87]\n\ts_add_u32 s86, s86, 0x2000\n\ts_addc_u32 s87, s87, 0\n\ts_mov_b32 m0, %[snk]\n\ts_add_u32 s90, %[wdo], 5120\n\t" \
    "buffer_load_dwordx4 %[l16], %[rd], s90 offen lds\n\tds_read_b128 v[68:71], v93 offset:4096\n\tds_read_b128 v[84:87], v94 offset:3072\n\tds_read_b128 v[88:91], v94 offset:3584\n\t" \
    "v_exp_f32 v34, v34\n\tv_exp_f32 v35, v35\n\tv_exp_f32 v36, v36\n\ts_waitcnt lgkmcnt(3)\n\tv_mfma_f32_32x32x16_bf16 v[48:63], v[72:75], v[64:67], v[16:31]\n\tv_exp_f32 v37, v37\n\t" \
    "v_exp_f32 v38, v38\n\tv_exp_f32 v39, v39\n\tv_exp_f32 v40, v40\n\tv_exp_f32 v41, v41\n\tv_exp_f32 v42, v42\n\tv_smfmac_f32_32x32x32_bf16 v[0:15], v[76:79], v[32:39], v92\n\t" \
    "v_exp_f32 v43, v43\n\tv_exp_f32 v44, v44\n\tv_exp_f32 v45, v45\n\tv_exp_f32 v46, v46\n\tv_exp_f32 v47, v47\n\tv_exp_f32 v48, v48\n\tv_exp_f32 v49, v49\n\t" \
    "v_smfmac_f32_32x32x32_bf16 v[0:15], v[80:83], v[40:47], v92\n\ts_mov_b32 m0, s89\n\ts_add_u32 s89, s89, 1024\n\tglobal_load_lds_dwordx4 %[xv], s[86:87]\n\t" \
    "s_add_u32 s86, s86, 0x2000\n\ts_addc_u32 s87, s87, 0\n\ts_mov_b32 m0, %[snk]\n\ts_add_u32 s90, %[wdo], 6144\n\tbuffer_load_dwordx4 %[l16], %[rd], s90 offen lds\n\t" \
    "v_add_u32 v94, v94, v95\n\tds_read_b128 v[72:75], v93 offset:5120\n\tds_read_b128 v[76:79], v94 offset:0\n\tds_read_b128 v[80:83], v94 offset:512\n\tv_exp_f32 v50, v50\n\t" \
    "v_exp_f32 v51, v51\n\tv_exp_f32 v52, v52\n\ts_waitcnt lgkmcnt(3)\n\tv_mfma_f32_32x32x16_bf16 v[32:47], v[68:71], v[64:67], v[16:31]\n\tv_exp_f32 v53, v53\n\tv_exp_f32 v54, v54\n\t" \
    "v_exp_f32 v55, v55\n\tv_exp_f32 v56, v56\n\tv_exp_f32 v57, v57\n\tv_exp_f32 v58, v58\n\tv_smfmac_f32_32x32x32_bf16 v[0:15], v[84:87], v[48:55], v92\n\tv_exp_f32 v59, v59\n\t" \
    "v_exp_f32 v60, v60\n\tv_exp_f32 v61, v61\n\tv_exp_f32 v62, v62\n\tv_exp_f32 v63, v63\n\tv_add_u32 v93, 0x1000, v93\n\ts_setprio 2\n\tv_exp_f32 v32, v32\n\tv_exp_f32 v33, v33\n\t" \
    "v_smfmac_f32_32x32x32_bf16 v[0:15], v[88:91], v[56:63], v92\n\tbuffer_load_dwordx4 v[224:227], %[l16], %[rs], %[foo] offen offset:0 sc1\n\t" \
    "ds_read_b128 v[68:71], v93 offset:2048\n\tds_read_b128 v[84:87], v94 offset:1024\n\tds_read_b128 v[88:91], v94 offset:1536\n\tv_exp_f32 v34, v34\n\tv_exp_f32 v35, v35\n\t" \
    "v_exp_f32 v36, v36\n\ts_waitcnt lgkmcnt(3)\n\tv_mfma_f32_32x32x16_bf16 v[48:63], v[72:75], v[64:67], v[16:31]\n\tv_exp_f32 v37, v37\n\tv_exp_f32 v38, v38\n\tv_exp_f32 v39, v39\n\t" \
    "v_exp_f32 v40, v40\n\tv_exp_f32 v41, v41\n\tv_exp_f32 v42, v42\n\tv_smfmac_f32_32x32x32_bf16 v[0:15], v[76:79], v[32:39], v92\n\tv_exp_f32 v43, v43\n\tv_exp_f32 v44, v44\n\t" \
    "v_exp_f32 v45, v45\n\tv_exp_f32 v46, v46\n\tv_exp_f32 v47, v47\n\tv_exp_f32 v48, v48\n\tv_exp_f32 v49, v49\n\tv_smfmac_f32_32x32x32_bf16 v[0:15], v[80:83], v[40:47], v92\n\t" \
    "buffer_load_dwordx4 v[228:231], %[l16], %[rs], %[foo] offen offset:1024 sc1\n\tds_read_b128 v[72:75], v93 offset:3072\n\tds_read_b128 v[76:79], v94 offset:2048\n\t" \
    "ds_read_b128 v[80:83], v94 offset:2560\n\tv_exp_f32 v50, v50\n\tv_exp_f32 v51, v51\n\tv_exp_f32 v52, v52\n\ts_waitcnt lgkmcnt(3)\n\t" \
    "v_mfma_f32_32x32x16_bf16 v[32:47], v[68:71], v[64:67], v[16:31]\n\tv_exp_f32 v53, v53\n\tv_exp_f32 v54, v54\n\tv_exp_f32 v55, v55\n\tv_exp_f32 v56, v56\n\tv_exp_f32 v57, v57\n\t" \
    "v_exp_f32 v58, v58\n\tv_smfmac_f32_32x32x32_bf16 v[0:15], v[84:87], v[48:55], v92\n\tv_exp_f32 v59, v59\n\tv_exp_f32 v60, v60\n\tv_exp_f32 v61, v61\n\tv_exp_f32 v62, v62\n\t" \
    "v_exp_f32 v63, v63\n\ts_setprio 1\n\tv_exp_f32 v32, v32\n\tv_exp_f32 v33, v33\n\tv_smfmac_f32_32x32x32_bf16 v[0:15], v[88:91], v[56:63], v92\n\t" \
    "buffer_load_dwordx4 v[232:235], %[l16], %[rs], %[foo] offen offset:2048 sc1\n\tds_read_b128 v[68:71], v93 offset:4096\n\tds_read_b128 v[84:87], v94 offset:3072\n\t" \
    "ds_read_b128 v[88:91], v94 offset:3584\n\tv_exp_f32 v34, v34\n\tv_exp_f32 v35, v35\n\tv_exp_f32 v36, v36\n\ts_waitcnt lgkmcnt(3)\n\t" \
    "v_mfma_f32_32x32x16_bf16 v[48:63], v[72:75], v[64:67], v[16:31]\n\tv_exp_f32 v37, v37\n\tv_exp_f32 v38, v38\n\tv_exp_f32 v39, v39\n\tv_exp_f32 v40, v40\n\tv_exp_f32 v41, v41\n\t" \
    "v_exp_f32 v42, v42\n\tv_smfmac_f32_32x32x32_bf16 v[0:15], v[76:79], v[32:39], v92\n\tv_exp_f32 v43, v43\n\tv_exp_f32 v44, v44\n\tv_exp_f32 v45, v45\n\tv_exp_f32 v46, v46\n\t" \
    "v_exp_f32 v47, v47\n\tv_exp_f32 v48, v48\n\tv_exp_f32 v49, v49\n\tv_smfmac_f32_32x32x32_bf16 v[0:15], v[80:83], v[40:47], v92\n\t" \
    "buffer_load_dwordx4 v[236:239], %[l16], %[rs], %[foo] offen offset:3072 sc1\n\tv_add_u32 v94, v94, v95\n\tds_read_b128 v[72:75], v93 offset:5120\n\t" \
    "ds_read_b128 v[76:79], v94 offset:0\n\tds_read_b128 v[80:83], v94 offset:512\n\tv_exp_f32 v50, v50\n\tv_exp_f32 v51, v51\n\tv_exp_f32 v52, v52\n\ts_waitcnt lgkmcnt(3)\n\t" \
    "v_mfma_f32_32x32x16_bf16 v[32:47], v[68:71], v[64:67], v[16:31]\n\tv_exp_f32 v53, v53\n\tv_exp_f32 v54, v54\n\tv_exp_f32 v55, v55\n\tv_exp_f32 v56, v56\n\tv_exp_f32 v57, v57\n\t" \
    "v_exp_f32 v58, v58\n\tv_smfmac_f32_32x32x32_bf16 v[0:15], v[84:87], v[48:55], v92\n\tv_exp_f32 v59, v59\n\tv_exp_f32 v60, v60\n\tv_exp_f32 v61, v61\n\tv_exp_f32 v62, v62\n\t" \
    "v_exp_f32 v63, v63\n\tv_add_u32 v93, 0x1000, v93\n\ts_nop 1\n\tv_smfmac_f32_32x32x32_bf16 v[0:15], v[88:91], v[56:63], v92\n\ts_waitcnt lgkmcnt(0)\n\ts_nop 7\n\ts_nop 7\n\t" \
    "s_mov_b32 m0, s88\n\ts_setprio 0\n\t"
#define ATT_CLOB "v16", "v17", "v18", "v19", "v20", "v21", "v22", "v23", "v24", "v25", "v26", "v27", "v28", "v29", "v30", "v31", "v32", "v33", "v34", "v35", "v36", "v37", "v38", "v39", "v40", "v41", "v42", "v43", "v44", "v45", "v46", "v47", "v48", "v49", "v50", "v51", "v52", "v53", "v54", "v55", "v56", "v57", "v58", "v59", "v60", "v61", "v62", "v63", "v68", "v69", "v70", "v71", "v72", "v73", "v74", "v75", "v76", "v77", "v78", "v79", "v80", "v81", "v82", "v83", "v84", "v85", "v86", "v87", "v88", "v89", "v90", "v91", "v92", "v93", "v94", "v95", "s86", "s87", "s88", "s89", "s90", "s91", "vcc", "scc", "memory"
#define ATT_CLOB_X "v160", "v161", "v162", "v163"

typedef __attribute__((ext_vector_type(32))) unsigned u32x32;
typedef __attribute__((ext_vector_type(16))) unsigned u32x16;
#define SUB4(v, i) __builtin_shufflevector(v, v, 4 * (i), 4 * (i) + 1, 4 * (i) + 2, 4 * (i) + 3)
#define PRIO(n) __builtin_amdgcn_s_setprio(n)
#define BAR() asm volatile("s_waitcnt lgkmcnt(0)\n\ts_barrier" ::: "memory")

template <int N>
__device__ __forceinline__ void load_frags(u32x4 (&f)[N], rsrc_t rs, unsigned voff, unsigned soff) {
#pragma unroll
    for (int i = 0; i < N; ++i) f[i] = ld_sc1(rs, voff + (i & 3) * 1024, soff + (i >> 2) * 4096);
}
template <int OFF> __device__ __forceinline__ void lda(u32x4& f, rsrc_t rs, unsigned voff, unsigned soff) {
    asm volatile("buffer_load_dwordx4 %0, %1, %2, %3 offen offset:%4 sc1" : "=v"(f) : "v"(voff), "s"(rs), "s"(soff), "n"(OFF) : "memory");
}
template <int B, int E, int N> __device__ __forceinline__ void lda_frags(u32x4 (&f)[N], rsrc_t rs, unsigned voff, unsigned soff) {
    if constexpr (B < E) { lda<(B & 3) * 1024>(f[B], rs, voff, soff + (B >> 2) * 4096); lda_frags<B + 1, E>(f, rs, voff, soff); }
}
template <int N> __device__ __forceinline__ void pin_frags(u32x4 (&f)[N]) {
#pragma unroll
    for (int i = 0; i < N; ++i) asm volatile("" : "+v"(f[i]));
}
#define WAITV(n) asm volatile("s_waitcnt vmcnt(" #n ")" ::: "memory")
template <int N>
__device__ __forceinline__ bool pending(const u32x4 (&f)[N]) {
    unsigned m = 0;
#pragma unroll
    for (int i = 0; i < N; ++i) m = umax4(m, f[i]);
    return __any(m == SENT);
}
template <int N>
__device__ __forceinline__ void settle_frags(u32x4 (&f)[N], rsrc_t rs, unsigned voff, unsigned soff) {
    if (pending<N>(f)) {
        int spin = 0;
#pragma nounroll
        do {
            __builtin_amdgcn_s_sleep(2);
            asm volatile("" ::: "memory");
            load_frags<N>(f, rs, voff, soff);
            ++spin;
        } while (pending<N>(f) & (spin < SPIN_MAX));
    }
}
#define PIN2(a, b) asm volatile("" : "+v"(a), "+v"(b))
__device__ __forceinline__ void put_partials(unsigned base0, unsigned base1, const f32x16& c0, const f32x16& c1) {
#pragma unroll
    for (int g = 0; g < 4; ++g) {
        *(LAS f32x4*)(base0 + g * 1024) = f32x4{c0[4 * g], c0[4 * g + 1], c0[4 * g + 2], c0[4 * g + 3]};
        *(LAS f32x4*)(base1 + g * 1024) = f32x4{c1[4 * g], c1[4 * g + 1], c1[4 * g + 2], c1[4 * g + 3]};
    }
}

__device__ __forceinline__ void cnt_wait(const unsigned* p) {
    unsigned c = __hip_atomic_load(p, __ATOMIC_RELAXED, __HIP_MEMORY_SCOPE_AGENT);
    if (!__all(c >= 32u)) {
        int spin = 0;
#pragma nounroll
        do {
            __builtin_amdgcn_s_sleep(8);
            c = __hip_atomic_load(p, __ATOMIC_RELAXED, __HIP_MEMORY_SCOPE_AGENT);
            ++spin;
        } while (!__all(c >= 32u) & (spin < SPIN_MAX));
    }
    asm volatile("" ::: "memory");
}
template <int I> struct IC { static constexpr int v = I; };
template <int B, int E, class F> __device__ __forceinline__ void sfor(F&& f) { if constexpr (B < E) { f(IC<B>{}); sfor<B + 1, E>(f); } }
#define PIN1(a) asm volatile("" : "+v"(a))
__device__ __forceinline__ u32x4 lds_tr2(unsigned a) { const u32x2 lo = lds_tr(a), hi = lds_tr(a + 1024); return u32x4{lo[0], lo[1], hi[0], hi[1]}; }
template <int N>
__device__ __forceinline__ void probe(rsrc_t rs, unsigned soff) {
    const unsigned voff = ((threadIdx.x & 63) % N) * 1024 + 1020;
    unsigned c = __builtin_amdgcn_raw_buffer_load_b32(rs, voff, soff, 16);
    if (__any(c == SENT)) {
        int spin = 0;
#pragma nounroll
        do {
            __builtin_amdgcn_s_sleep(PROBE_SLEEP);
            asm volatile("" ::: "memory");
            c = __builtin_amdgcn_raw_buffer_load_b32(rs, voff, soff, 16);
            ++spin;
        } while (__any(c == SENT) & (spin < SPIN_MAX));
    }
    asm volatile("" ::: "memory");
}
__device__ __forceinline__ float sum_rows16(float v) {
    auto t = __builtin_amdgcn_permlane16_swap(__float_as_uint(v), __float_as_uint(v), false, false);
    return __uint_as_float(t[0]) + __uint_as_float(t[1]);
}

__global__ __launch_bounds__(512, 2) void k2_mega(char* __restrict__ ws, const unsigned* __restrict__ nq, const u16* __restrict__ x, const u16* __restrict__ wo,
                                                  const u16* __restrict__ wg, const u16* __restrict__ wu, const u16* __restrict__ wd, u16* __restrict__ out) {
    __shared__ __attribute__((aligned(16))) char lds[L_END];
    const unsigned lds0 = (unsigned)(uintptr_t)lds;
    const int b = blockIdx.x, xc = b & 7, bi = b >> 3;
    const int head = xc * 4 + (bi & 3), qb = bi >> 2;
    const int mi = bi & 3, ni = bi >> 2;
    const int rtP = ((xc >> 1) * 2 + ((bi >> 1) & 1)) * 8 + 2 * (bi & 1), fs = (xc & 1) * 8 + (bi >> 2);
    const int lane = threadIdx.x & 63, w = rfl(threadIdx.x >> 6), r = lane & 31, h = lane >> 5;
    const int kvh = w >> 2, qi = w & 3;
    const unsigned l16 = lane * 16;
    STAMP_AT(ws, 1, 0);

    u32x4 qA, qB; unsigned nk;
    const char* kvsrc = ws + ((w & 1) ? WS_V : WS_K) + (size_t)head * 65536 + ((w >> 1) & 1) * 32768 + (w >> 2) * 1024;
    const unsigned kvdst = lds0 + ((w & 1) ? L_V : L_K) + ((w >> 1) & 1) * 32768 + (w >> 2) * 1024;
    {
        const char* qp = ws + WS_Q + ((size_t)head * 64 + qb * 8 + qi) * 1024;
#pragma unroll
        for (int i = 0; i < 3; ++i) glds16(kvsrc + i * 2048, l16, kvdst + i * 2048);
        asm volatile("global_load_dwordx4 %0, %3, %4\n\tglobal_load_dwordx4 %1, %3, %5\n\tglobal_load_dword %2, %6, %7"
                     : "=&v"(qA), "=&v"(qB), "=&v"(nk) : "v"(l16), "s"(qp), "s"(qp + 4096), "v"(lane * 4), "s"(ws + WS_NK + head * 256) : "memory");
    }
    const rsrc_t rs = __builtin_amdgcn_make_buffer_rsrc(ws, 0, (int)WS_END, 0x00020000);
    { const unsigned one = (((threadIdx.x >> 1) & 11) == 0) ? 0x3f803f80u : 0u; *(LAS u32x2*)(lds0 + L_ONE + threadIdx.x * 8) = u32x2{one, one}; }
    unsigned qm = 0;
#pragma unroll
    for (int i = 0; i < 8; ++i) qm = max(qm, nq[head * 64 + qb * 8 + i]);
    const unsigned ka0 = lds0 + L_K + kvh * 32768 + l16;
    const unsigned va0 = (r < 16) ? (lds0 + L_V + kvh * 32768 + h * 256 + r * 16) : (lds0 + L_ONE + h * 256 + (r & 15) * 16);
    const unsigned vs0 = (r < 16) ? 4096u : 0u;
#pragma unroll
    for (int i = 3; i < 8; ++i) glds16(kvsrc + i * 2048, l16, kvdst + i * 2048);
    const int rtA = xc * 8 + mi, rtB = rtA + 4;
    const int fct = (w >> 1) & 1, fj = w & 1;
    const int frt = (w < 4) ? rtA : rtB, fks = ni * 4 + fct * 2 + fj;
    const unsigned apo = (unsigned)WS_A + (rtP * 112 + fs * 7 + w) * 1024;
    const char* wga = (const char*)((lane & 2) ? wu : wg) + (size_t)(lane >> 2) * (FF * 2) + (fs * 7 + (w < 7 ? w : 0)) * 32 + (lane & 1) * 16;
    const unsigned wld = lds0 + L_R + (w < 7 ? w : 0) * 4096;
    unsigned wta = wld + (4 * h + ((lane & 15) >> 2)) * 64 + (lane & 3) * 8 + ((lane >> 4) & 1) * 32;
    const unsigned flg = w < 7 ? 4u : 0u;
    asm volatile("" : "+v"(wga), "+v"(wta));
    asm volatile("s_waitcnt vmcnt(5) lgkmcnt(0)\n\ts_barrier" : "+v"(qA), "+v"(qB), "+v"(nk) :: "memory");
    STAMP_AT(ws, 1, 1);
    float mA = 0.f, mB = 0.f;
    if (!__all(__uint_as_float(nk) * __uint_as_float(qm) <= 1600.f)) {
#pragma unroll
        for (int i = 8; i < 16; ++i) glds16(kvsrc + i * 2048, l16, kvdst + i * 2048);
        asm volatile("s_waitcnt vmcnt(0)\n\ts_barrier" ::: "memory");
        mA = -3.0e38f; mB = -3.0e38f;
        for (int t = 0; t < 64; ++t) {
            const u32x4 kf = lds_ld128(lds0 + L_K + t * 1024 + l16);
            mA = fmaxf(mA, max16(mfma32(kf, qA, f32x16{}))); mB = fmaxf(mB, max16(mfma32(kf, qB, f32x16{})));
        }
        mA = max_halves(mA); mB = max_halves(mB);
        BAR();
    }
    f32x16 oacc; u32x32 wq0, wq1, wq2, wq3; u32x16 foq;
    {
#define XS_OUT
        STAMP_AT(ws, 1, 2);
        asm volatile(ATT_STAGE_A : "=&{v[0:15]}"(oacc), "=&{v[96:127]}"(wq0), "=&{v[128:159]}"(wq1), [wga] "+v"(wga) XS_OUT
                     : "{v[64:67]}"(qA), [negm] "v"(-mA), [ka] "v"(ka0), [va] "v"(va0), [vs] "v"(vs0), [l16] "v"(l16), [kvs] "s"(kvsrc + 16384), [kvd] "s"(kvdst + 16384),
                       [wld] "s"(wld), [wta] "v"(wta), [flg] "s"(flg), [rs] "s"(rs), [pa] "s"(apo)
                     : ATT_CLOB, ATT_CLOB_X);
    }
    const unsigned xch = lds0 + L_R + qi * 4096 + lane * 4;
    auto finish_o = [&](int rt) {
        float o[9];
#pragma unroll
        for (int i = 0; i < 9; ++i) o[i] = oacc[i] + *(const LAS float*)(xch + i * 256);
        const float rl = __builtin_amdgcn_rcpf(o[8]);
        u32x4 t;
#pragma unroll
        for (int e = 0; e < 4; ++e) t[e] = clean(pk_bf16(o[2 * e] * rl, o[2 * e + 1] * rl));
        st_sc1(rs, l16, (unsigned)WS_O + (rt * 32 + head) * 1024, t);
    };
    if (kvh == 1) {
#pragma unroll
        for (int i = 0; i < 9; ++i) *(LAS float*)(xch + i * 256) = oacc[i];
    }
    STAMP_AT(ws, 1, 7);
    BAR();
    if (kvh == 0) finish_o(qb * 8 + qi);
    STAMP_AT(ws, 1, 8);
    const unsigned xv = (lane >> 3) * 1024 + (lane & 7) * 16;
    asm volatile(ATT_STAGE_B : "=&{v[0:15]}"(oacc), "+{v[96:127]}"(wq0), "+{v[128:159]}"(wq1), "=&{v[160:191]}"(wq2), "=&{v[192:223]}"(wq3), "=&{v[224:239]}"(foq), [wga] "+v"(wga)
                 : "{v[64:67]}"(qB), [negm] "v"(-mB), [ka] "v"(ka0), [va] "v"(va0), [vs] "v"(vs0), [l16] "v"(l16), [rs] "s"(rs), [flg] "s"(flg), [wld] "s"(wld), [wta] "v"(wta),
                   [wos] "s"((const char*)wo + (size_t)w * 65536 + ni * 128), [wod] "s"(lds0 + piece(4 * w)), [foo] "s"((unsigned)WS_O + (rtA * 32 + 4 * w) * 1024),
                   [rd] "s"(__builtin_amdgcn_make_buffer_rsrc((void*)wd, 0, FF * DM * 2, 0x00020000)), [wdo] "s"((bi * 56 + w * 7) * 1024), [snk] "s"(lds0 + L_SINK),
                   [xs] "s"((const char*)x + (size_t)(frt * 32 + qi * 8) * 1024 + ni * 128), [xv] "v"(xv), [xd] "s"(lds0 + L_R + 16384 + w * 1024),
                   [cso] "s"((unsigned)WS_CNT + (bi & 7) * 128)
                 : ATT_CLOB);
    asm volatile("s_waitcnt vmcnt(0)" : "+{v[224:239]}"(foq) :: "memory");
    u32x4 fo[4] = {SUB4(foq, 0), SUB4(foq, 1), SUB4(foq, 2), SUB4(foq, 3)};
    u32x4 wb[32];
#define WBX(q, n) wb[8 * n] = SUB4(q, 0); wb[8 * n + 1] = SUB4(q, 1); wb[8 * n + 2] = SUB4(q, 2); wb[8 * n + 3] = SUB4(q, 3); \
                  wb[8 * n + 4] = SUB4(q, 4); wb[8 * n + 5] = SUB4(q, 5); wb[8 * n + 6] = SUB4(q, 6); wb[8 * n + 7] = SUB4(q, 7);
    WBX(wq0, 0) WBX(wq1, 1) WBX(wq2, 2) WBX(wq3, 3)
    STAMP_AT(ws, 1, 9);
    u32x4 hres = {};
    auto gather8 = [&](unsigned off_lo, bool upper, float (&s)[8]) {
        f32x4 t[16];
#pragma unroll
        for (int v = 0; v < 8; ++v) {
            const unsigned p = upper ? lds0 + (v >> 1) * 32768 + 16384 + (v & 1) * 8192 + fct * 4096 : lds0 + piece(fct * 32 + 4 * v);
            t[2 * v] = lds_ldf4(p + off_lo); t[2 * v + 1] = lds_ldf4(p + off_lo + 1024);
        }
#pragma unroll
        for (int e = 0; e < 4; ++e) {
            s[e] = ((t[0][e] + t[2][e]) + (t[4][e] + t[6][e])) + ((t[8][e] + t[10][e]) + (t[12][e] + t[14][e]));
            s[4 + e] = ((t[1][e] + t[3][e]) + (t[5][e] + t[7][e])) + ((t[9][e] + t[11][e]) + (t[13][e] + t[15][e]));
        }
    };
    const unsigned xa = lds0 + L_R + 16384 + kvh * 4096 + r * 128 + (w & 3) * 32 + 8 * h;
    auto finish_h1 = [&](bool upper) {
        const u32x2 x0 = *(const LAS u32x2*)xa, x1 = *(const LAS u32x2*)(xa + 16);
        float s[8]; gather8(fj * 2048 + l16, upper, s);
        hres = u32x4{clean(pk_bf16(s[0] + bf_lo(x0[0]), s[1] + bf_hi(x0[0]))), clean(pk_bf16(s[2] + bf_lo(x0[1]), s[3] + bf_hi(x0[1]))),
                     clean(pk_bf16(s[4] + bf_lo(x1[0]), s[5] + bf_hi(x1[0]))), clean(pk_bf16(s[6] + bf_lo(x1[1]), s[7] + bf_hi(x1[1])))};
        st_sc1(rs, l16, (unsigned)WS_H + (frt * 32 + fks) * 1024, hres);
    };
    auto finish_h2 = [&]() {
        float sx = 0.f, sxx = 0.f;
#pragma unroll
        for (int e = 0; e < 4; ++e) { sx = dot2(hres[e], 0x3f803f80u, sx); sxx = dot2(hres[e], hres[e], sxx); }
        sx = sum_halves(sx); sxx = sum_halves(sxx);
        __builtin_amdgcn_raw_buffer_store_b32(clean(__float_as_uint(h ? sxx : sx)), rs, r * 8 + h * 4, (unsigned)WS_ST + (frt * 32 + fks) * 256, 16);
    };
    const unsigned upo = lds0 + (w >> 1) * 32768 + 16384 + (w & 1) * 8192 + l16;
    const unsigned lo0 = lds0 + piece(4 * w) + l16, lo1 = lds0 + piece(32 + 4 * w) + l16;
    const unsigned hao = (unsigned)WS_H + (rtP * 32 + 8 * w) * 1024, hbo = hao + 4 * 32 * 1024;
    const unsigned trl = (4 * h + ((lane & 15) >> 2)) * 128 + ((lane >> 4) & 1) * 32 + (lane & 3) * 8;
    unsigned lb0 = lds0 + l16, lb1 = lds0 + 65536 + l16;
    PIN2(lb0, lb1);
    auto stats = [&](u32x4 (&sp)[8], int slot, unsigned so) {
        settle_frags<8>(sp, rs, l16, so);
        float s[4];
#pragma unroll
        for (int e = 0; e < 4; ++e) { s[e] = 0.f;
#pragma unroll
            for (int i = 0; i < 8; ++i) s[e] += __uint_as_float(sp[i][e]);
            s[e] = sum_halves(sum_rows16(s[e])); }
        const float mu0 = s[0] * (1.f / DM), v0 = fmaxf(s[1] * (1.f / DM) - mu0 * mu0, 0.f) + LN_EPS;
        const float mu1 = s[2] * (1.f / DM), v1 = fmaxf(s[3] * (1.f / DM) - mu1 * mu1, 0.f) + LN_EPS;
        if (lane < 16) *(LAS f32x4*)(lds0 + L_ST + slot * 256 + lane * 16) = f32x4{-mu0, v0, -mu1, v1};
    };
    auto stage8 = [&](unsigned so, unsigned dst) {
#pragma unroll
        for (int i = 0; i < 8; ++i) glds16_sc1(ws + so + i * 1024, l16, dst + i * 1024);
    };
    auto verify8 = [&](unsigned so, unsigned dst) {
        int spin = 0; bool bad;
#pragma nounroll
        do {
            unsigned m = 0;
#pragma unroll
            for (int i = 0; i < 8; ++i) m = umax4(m, lds_ld128(dst + l16 + i * 1024));
            bad = __any(m == SENT) & (++spin < SPIN_MAX);
            if (bad) { probe<8>(rs, so); stage8(so, dst); asm volatile("s_waitcnt vmcnt(0)" ::: "memory"); }
        } while (bad);
    };
    const unsigned hbd = lds0 + piece(8 * w), had = hbd + 16384;
    {
        f32x16 c0, c1;
        auto p3mm = [&](unsigned oo) {
            u32x4 bw[8];
#pragma unroll
            for (int k = 0; k < 4; ++k) { const unsigned ta = lds0 + piece(4 * w) + trl + (k >> 1) * 65536 + (k & 1) * 2048; bw[k] = lds_tr2(ta); bw[4 + k] = lds_tr2(ta + 64); }
            settle_frags<4>(fo, rs, l16, oo);
            c0 = f32x16{}; c1 = f32x16{};
#pragma unroll
            for (int k = 0; k < 4; ++k) { c0 = mfma32(bw[k], fo[k], c0); c1 = mfma32(bw[4 + k], fo[k], c1); }
            PIN2(c0, c1);
        };
        const unsigned oao = (unsigned)WS_O + (rtA * 32 + 4 * w) * 1024, obo = oao + 4 * 32 * 1024;
        if (kvh == 1) {
#pragma unroll
            for (int i = 0; i < 9; ++i) *(LAS float*)(xch + i * 256) = oacc[i];
            BAR();
            p3mm(oao);
        } else {
            p3mm(oao);
            BAR();
            finish_o(qb * 8 + 4 + qi);
        }
        STAMP_AT(ws, 1, 10);
        if (threadIdx.x < 16) *(LAS unsigned*)(lds0 + L_FLAG + threadIdx.x * 4) = 0u;
        put_partials(upo, upo + 4096, c0, c1);
        BAR();
        STAMP_AT(ws, 1, 11);
        if (w < 4) finish_h1(true);
        lda_frags<0, 4>(fo, rs, l16, obo);
        if (w < 4) finish_h2();
        STAMP_AT(ws, 1, 12);
        auto colsum = [&]() {
            float c4[4] = {0.f, 0.f, 0.f, 0.f};
#pragma unroll
            for (int k = 0; k < 32; ++k)
#pragma unroll
                for (int e = 0; e < 4; ++e) c4[e] = dot2(wb[k][e], 0x3f803f80u, c4[e]);
            const float t = sum_halves((c4[0] + c4[1]) + (c4[2] + c4[3]));
            if (h == 0) *(LAS float*)(lds0 + L_CS + (w * 32 + r) * 4) = t;
        };
        if (w == 7) cnt_wait((const unsigned*)(ws + WS_CNT + xc * 1024 + (lane & 7) * 128));
        else if (w >= 4) colsum();
        if (w < 4) WAITV(1); else WAITV(0);
        pin_frags<4>(fo);
        p3mm(obo);
        STAMP_AT(ws, 1, 13);
        put_partials(lo0, lo1, c0, c1);
        BAR();
        STAMP_AT(ws, 1, 3);
        stage8(hao, had);
        STAMP_AT(ws, 1, 4);
        if (w >= 4) {
            finish_h1(false); finish_h2();
            WAITV(2);
        } else {
            u32x4 sp[8];
            const unsigned so = (unsigned)WS_ST + (rtP + (w & 1)) * 8192;
            lda_frags<0, 8>(sp, rs, l16, so);
            colsum();
            WAITV(0);
            pin_frags<8>(sp);
            if (w < 2) stats(sp, w, so);
        }
        STAMP_AT(ws, 1, 14);
        verify8(hao, had);
        STAMP_AT(ws, 1, 15);
        BAR();
    }
    STAMP_AT(ws, 1, 16);
    u32x4 wdv[28];
    u32x4 fa[14];
    const unsigned aoA = (unsigned)WS_A + (rtA * 112 + 14 * w) * 1024, aoB = (unsigned)WS_A + (rtB * 112 + 14 * w) * 1024;
    const char* wdb = (const char*)wd + (size_t)w * 224 * 1024 + ni * 128;
    const unsigned wdr = lds0 + (w >> 1) * 32768 + 16384 + (w & 1) * 8192;
    auto wd_slot = [&](int j) { const int sl = j % 12; return sl < 4 ? wld + sl * 1024 : wdr + (sl - 4) * 1024; };
    auto wd_issue = [&](int j) { if (!X_NOWD) glds16(wdb + j * 8192, xv, wd_slot(j)); };
    auto wd_take = [&](int i) { wdv[2 * i] = lds_tr2(wd_slot(2 * i) + trl); wdv[2 * i + 1] = lds_tr2(wd_slot(2 * i) + trl + 64); };
    if (w == 7) {
        u32x4 sp0[8], sp1[8];
        STAMP_AT(ws, 1, 17);
#pragma unroll
        for (int j = 0; j < 28; ++j) wdv[j] = *(const u32x4*)(wdb + j * 8192 + xv);
        probe<8>(rs, hbo);
        stage8(hbo, hbd);
        const unsigned so = (unsigned)WS_ST + (rtP + 4) * 8192;
        lda_frags<0, 8>(sp0, rs, l16, so); lda_frags<0, 8>(sp1, rs, l16, so + 8192);
        asm volatile("s_waitcnt vmcnt(16)" ::: "memory");
        verify8(hbo, hbd);
        STAMP_AT(ws, 1, 18);
        BAR();
        STAMP_AT(ws, 1, 20);
        WAITV(0); pin_frags<8>(sp0); pin_frags<8>(sp1);
        stats(sp0, 2, so); stats(sp1, 3, so + 8192); lds_signal(lds0 + L_FLAG);
#pragma unroll
        for (int g = 0; g < 4; ++g) {
#pragma unroll
            for (int j = 0; j < 8; ++j) if (g * 8 + j < 28) *(LAS u32x4*)(wdr + j * 1024 + l16) = wdv[g * 8 + j];
            asm volatile("s_waitcnt lgkmcnt(0)" ::: "memory");
#pragma unroll
            for (int i = 0; i < 4; ++i) if (g * 4 + i < 14) { wdv[2 * (g * 4 + i)] = lds_tr2(wdr + i * 2048 + trl); wdv[2 * (g * 4 + i) + 1] = lds_tr2(wdr + i * 2048 + trl + 64); }
            asm volatile("s_waitcnt lgkmcnt(0)" ::: "memory");
        }
        probe<14>(rs, aoA);
        lda_frags<0, 14>(fa, rs, l16, aoA);
        WAITV(0); pin_frags<14>(fa);
    } else {
        constexpr float C1 = -2.0f * 0.7978845608028654f * 1.4426950408889634f, C2 = C1 * 0.044715f;
        f32x2 nmu, k1, k2, vv; u32x4 av; f32x4 csr[4];
        auto epi_begin = [&](int slot) {
            const f32x2 st = *(const LAS f32x2*)(lds0 + L_ST + (slot * 32 + r) * 8);
            const float rstd = __builtin_amdgcn_rsqf(st[1]);
            nmu = f32x2{st[0], st[0]}; vv = f32x2{st[1], st[1]};
            const float a1 = C1 * rstd, a2 = C2 * rstd * rstd * rstd;
            k1 = f32x2{a1, a1}; k2 = f32x2{a2, a2};
            PIN2(nmu, vv); PIN2(k1, k2);
        };
        auto epi_pair = [&](const f32x16& acc, int p) {
            const f32x2 cg = {csr[p >> 1][2 * (p & 1)], csr[p >> 1][2 * (p & 1) + 1]}, cu = {csr[2 + (p >> 1)][2 * (p & 1)], csr[2 + (p >> 1)][2 * (p & 1) + 1]};
            const f32x2 g = __builtin_elementwise_fma(nmu, cg, f32x2{acc[2 * p], acc[2 * p + 1]});
            const f32x2 u = __builtin_elementwise_fma(nmu, cu, f32x2{acc[8 + 2 * p], acc[9 + 2 * p]});
            const f32x2 z = g * __builtin_elementwise_fma(g * g, k2, k1);
            const f32x2 e = {__builtin_amdgcn_exp2f(z[0]), __builtin_amdgcn_exp2f(z[1])};
            const f32x2 d = __builtin_elementwise_fma(e, vv, vv);
            const f32x2 ri = {__builtin_amdgcn_rcpf(d[0]), __builtin_amdgcn_rcpf(d[1])};
            const f32x2 o = (g * u) * ri;
            av[p] = clean(pk_bf16(o[0], o[1]));
        };
        float eg, eu, ez, eo[2], snm, svv, sk1, sk2;
        auto epi_sbegin = [&](int slot) {
            const f32x2 st = *(const LAS f32x2*)(lds0 + L_ST + (slot * 32 + r) * 8);
            const float rstd = __builtin_amdgcn_rsqf(st[1]);
            snm = st[0]; svv = st[1]; sk1 = C1 * rstd; sk2 = C2 * rstd * rstd * rstd;
        };
        auto epi_step = [&](const f32x16& acc, int e, int q) {
            if (q == 0) { eg = fmaf(snm, csr[e >> 2][e & 3], acc[e]); PIN1(eg); eu = fmaf(snm, csr[2 + (e >> 2)][e & 3], acc[8 + e]); PIN1(eu); ez = eg * eg; PIN1(ez); }
            if (q == 1) { ez = fmaf(ez, sk2, sk1); PIN1(ez); eu = eg * eu; PIN1(eu); ez = eg * ez; PIN1(ez); }
            if (q == 2) { ez = __builtin_amdgcn_exp2f(ez); PIN1(ez); ez = fmaf(ez, svv, svv); PIN1(ez); }
            if (q == 3) { ez = __builtin_amdgcn_rcpf(ez); PIN1(ez); eo[e & 1] = eu * ez; PIN1(eo[e & 1]); if (e & 1) { av[e >> 1] = clean(pk_bf16(eo[0], eo[1])); PIN1(av[e >> 1]); } }
        };
#pragma unroll
        for (int g = 0; g < 4; ++g) csr[g] = lds_ldf4(lds0 + L_CS + (w * 32 + 8 * g + 4 * h) * 4);
        PRIO(2);
        f32x16 c0 = {}, c1 = {}, c2 = {}, c3 = {};
        constexpr int RD = 5;
        u32x4 ar[RD + 1];
        auto adrA = [&](int i) { return (i < 32 ? lb0 : lb1) + piece(i & 31) + 16384; };
        auto adrB = [&](int i) { return ((i & 1) ? lb1 : lb0) + piece(i >> 1); };
#pragma unroll
        for (int i = 0; i < RD; ++i) ar[i] = lds_ld128(adrA(i));
#pragma unroll
        for (int j = 0; j < 4; ++j) wd_issue(j);
        sfor<0, 32>([&](auto I) { constexpr int i = I.v;
            ar[(i + RD) % (RD + 1)] = lds_ld128(adrA(i + RD));
            c0 = mfma32(wb[i], ar[i % (RD + 1)], c0);
            __builtin_amdgcn_sched_barrier(0);
        });
        PIN1(c0);
        STAMP_AT(ws, 1, 17);
        stage8(hbo, hbd);
        epi_sbegin(0);
        sfor<32, 64>([&](auto I) { constexpr int i = I.v;
            if (i + RD < 64) ar[(i + RD) % (RD + 1)] = lds_ld128(adrA(i + RD));
            c1 = mfma32(wb[i - 32], ar[i % (RD + 1)], c1);
            epi_step(c0, (i - 32) >> 2, i & 3);
            __builtin_amdgcn_sched_barrier(0);
        });
        PIN1(c1);
        asm volatile("s_waitcnt vmcnt(0)" ::: "memory");
        st_sc1(rs, l16, apo, av);
        verify8(hbo, hbd);
        STAMP_AT(ws, 1, 19);
        BAR();
        STAMP_AT(ws, 1, 20);
#pragma unroll
        for (int i = 0; i < RD; ++i) ar[i] = lds_ld128(adrB(i));
        epi_sbegin(1);
        constexpr int WD0 = 10, WDS = 6;
        sfor<0, 64>([&](auto I) { constexpr int i = I.v;
            if constexpr (i < 8) wd_issue(4 + i);
            if constexpr (i >= WD0 && (i - WD0) % WDS == 0) { constexpr int rd = (i - WD0) / WDS;
                if (rd >= 1) { wd_issue(2 * rd + 10); wd_issue(2 * rd + 11); }
                asm volatile("s_waitcnt vmcnt(10)" ::: "memory"); wd_take(rd); }
            if constexpr (i >= WD0 && (i - WD0) % WDS == WDS - 1) PIN2(wdv[2 * ((i - WD0) / WDS)], wdv[2 * ((i - WD0) / WDS) + 1]);
            if (i + RD < 64) ar[(i + RD) % (RD + 1)] = lds_ld128(adrB(i + RD));
            if (i & 1) c3 = mfma32(wb[i >> 1], ar[i % (RD + 1)], c3); else c2 = mfma32(wb[i >> 1], ar[i % (RD + 1)], c2);
            if constexpr (i < 32 && !X_NOEPI) epi_step(c1, i >> 2, i & 3);
            if constexpr (i == 32) st_sc1(rs, l16, apo + 112 * 1024, av);
            if constexpr (i >= 59) lda_frags<i - 59, i - 58>(fa, rs, l16, aoA);
            __builtin_amdgcn_sched_barrier(0);
        });
        PIN2(c2, c3);
        STAMP_AT(ws, 1, 21);
        lds_wait_ge(lds0 + L_FLAG, 1);
        epi_begin(2);
#pragma unroll
        for (int p = 0; p < 4; ++p) epi_pair(c2, p);
        st_sc1(rs, l16, apo + 4 * 112 * 1024, av);
        epi_begin(3);
#pragma unroll
        for (int p = 0; p < 4; ++p) epi_pair(c3, p);
        st_sc1(rs, l16, apo + 5 * 112 * 1024, av);
        PRIO(0);
        lda_frags<5, 14>(fa, rs, l16, aoA);
        asm volatile("s_waitcnt vmcnt(16)" ::: "memory");
#pragma unroll
        for (int i = 9; i < 14; ++i) wd_take(i);
        WAITV(0); pin_frags<14>(fa);
        STAMP_AT(ws, 1, 22);
    }
    {
        if (pending<14>(fa)) { probe<14>(rs, aoA); load_frags<14>(fa, rs, l16, aoA); settle_frags<14>(fa, rs, l16, aoA); }
        STAMP_AT(ws, 1, 23);
        f32x16 c0 = {}, c1 = {};
        u32x4 fb[14];
        sfor<0, 14>([&](auto I) { constexpr int k = I.v;
            c0 = mfma32(wdv[2 * k], fa[k], c0); c1 = mfma32(wdv[2 * k + 1], fa[k], c1);
            __builtin_amdgcn_sched_barrier(0);
            lda_frags<k, k + 1>(fb, rs, l16, aoB);
            __builtin_amdgcn_sched_barrier(0);
        });
        PIN2(c0, c1);
        STAMP_AT(ws, 1, 24);
        put_partials(upo, upo + 4096, c0, c1);
        STAMP_AT(ws, 1, 25);
        BAR();
        auto finish_out = [&](bool upper) {
            float s[8]; gather8(fj * 2048 + l16, upper, s);
            unsigned o0 = pk_bf16(s[0] + bf_lo(hres[0]), s[1] + bf_hi(hres[0])), o1 = pk_bf16(s[2] + bf_lo(hres[1]), s[3] + bf_hi(hres[1]));
            unsigned o2 = pk_bf16(s[4] + bf_lo(hres[2]), s[5] + bf_hi(hres[2])), o3 = pk_bf16(s[6] + bf_lo(hres[3]), s[7] + bf_hi(hres[3]));
            auto s0 = __builtin_amdgcn_permlane32_swap(o0, o2, false, false), s1 = __builtin_amdgcn_permlane32_swap(o1, o3, false, false);
            *(u32x4*)(out + (size_t)(frt * 32 + r) * DM + fks * 16 + 8 * h) = u32x4{s0[0], s1[0], s0[1], s1[1]};
        };
        WAITV(0); pin_frags<14>(fb);
        if (pending<14>(fb)) { probe<14>(rs, aoB); load_frags<14>(fb, rs, l16, aoB); settle_frags<14>(fb, rs, l16, aoB); }
        STAMP_AT(ws, 1, 26);
        c0 = f32x16{}; c1 = f32x16{};
#pragma unroll
        for (int k = 0; k < 14; ++k) { c0 = mfma32(wdv[2 * k], fb[k], c0); c1 = mfma32(wdv[2 * k + 1], fb[k], c1); }
        put_partials(lo0, lo1, c0, c1);
        if (w < 4) finish_out(true);
        BAR();
        STAMP_AT(ws, 1, 27);
        if (w >= 4) finish_out(false);
    }
    STAMP_AT(ws, 1, 31);
}

extern "C" void kernel_launch(void* const* d_in, const int* in_sizes, int n_in, void* d_out, int out_size, void* d_ws, size_t ws_size,
                              hipStream_t stream) {
    const u16* x = (const u16*)d_in[0];
    const u16* wqkv = (const u16*)d_in[1];
    const u16* wo = (const u16*)d_in[2];
    const u16* wg = (const u16*)d_in[4];
    const u16* wu = (const u16*)d_in[5];
    const u16* wd = (const u16*)d_in[6];
    char* ws = (char*)d_ws;
    k1_qkv<<<256, 768, 0, stream>>>(x, wqkv, wo, wg, wu, wd, ws);
    k2_mega<<<256, 512, 0, stream>>>(ws, (const unsigned*)(ws + WS_NQ), x, wo, wg, wu, wd, (u16*)d_out);
}
```
